```python
import torch

def _softmax(x, axis=-1):
    return torch.softmax(x, dim=axis)
def _gelu(x):
    return torch.nn.functional.gelu(x, approximate="tanh")
def _silu(x):
    return torch.nn.functional.silu(x)
def _sigmoid(x):
    return torch.sigmoid(x)
def _layernorm(x, axis=-1, eps=1e-5):
    m = x.mean(dim=axis, keepdim=True)
    v = x.var(dim=axis, keepdim=True, unbiased=False)
    return (x - m) / torch.sqrt(v + eps)
def _scatter_add(tgt, idx, src, axis=0):
    return tgt.clone().index_add_(axis, idx, src)
def _segment_sum(src, seg, n):
    out = torch.zeros((n,) + src.shape[1:], dtype=src.dtype)
    return out.index_add_(0, seg, src)
def _pad(x, pw):
    flat = tuple(v for pair in reversed(pw) for v in pair)
    return torch.nn.functional.pad(x, flat)

kernel_name = 'recurrent_scan__spectral_gfnet__dense_activation_relu__prompt'

def setup_inputs():

    _w = (lambda a: (lambda t: t.to(torch.bfloat16) if t.is_floating_point() else t)(torch.as_tensor(a)))
    def _A_setup():
        B, S, D = 1, 2048, 512
        Df = D // 2 + 1
        return {
            "x": _w((lambda *s: torch.randn(*s, dtype=torch.bfloat16) * 0.05)(B, S, D)),
            "w_r": _w((lambda *s: torch.randn(*s, dtype=torch.bfloat16) * 0.1)(S, Df) * 0.02),
            "w_i": _w((lambda *s: torch.randn(*s, dtype=torch.bfloat16) * 0.1)(S, Df) * 0.02),
        }
    _A_in = _A_setup()

    def _B_setup():
        B, S, D, Dff = 1, 2048, 512, 1792
        return {
            "x": _w((lambda *s: torch.randn(*s, dtype=torch.bfloat16) * 0.05)(B, S, D)),
            "w1": _w((lambda *s: torch.randn(*s, dtype=torch.bfloat16) * 0.05)(D, Dff)),
            "w2": _w((lambda *s: torch.randn(*s, dtype=torch.bfloat16) * 0.05)(Dff, D)),
        }
    _B_in = _B_setup()

    return {
        "A_x": _A_in["x"],
        "A_w_r": _A_in["w_r"],
        "A_w_i": _A_in["w_i"],
        "B_x": _B_in["x"],
        "B_w1": _B_in["w1"],
        "B_w2": _B_in["w2"],
    }

def reference(A_x, A_w_r, A_w_i, B_x, B_w1, B_w2):
    def _A_ref(x, w_r, w_i):
        B, S, D = x.shape
        X = (lambda x, axes=(-2,-1): torch.fft.rfft2(x.float(), dim=axes))(x, axes=(-2, -1))
        W = w_r + 1j * w_i
        X = X * W
        y = (lambda x, s, axes=(-2,-1): torch.fft.irfft2(x, s=s, dim=axes).to(torch.bfloat16))(X, (S, D), axes=(-2, -1))
        return y

    def _B_ref(x, w1, w2):
        h = torch.einsum("bsd,df->bsf", x, w1)
        zero = torch.zeros_like(h)
        h = torch.maximum(h, zero)
        return torch.einsum("bsf,fd->bsd", h, w2)

    x = A_x
    xn = _layernorm(x, axis=-1)
    s = _A_ref(xn, A_w_r, A_w_i)
    g = _silu(_B_ref(xn, B_w1, B_w2))
    return x + s * g
```

```cpp
#include <hip/hip_runtime.h>
#include <stdint.h>
#include <utility>

#define DI __device__ __forceinline__
typedef __attribute__((ext_vector_type(8))) __bf16 bf16x8;
typedef __attribute__((ext_vector_type(4))) __bf16 bf16x4;
typedef __attribute__((ext_vector_type(2))) __bf16 bf16x2;
typedef __attribute__((ext_vector_type(4))) float f32x4;
typedef __attribute__((ext_vector_type(2))) float f32x2;
typedef __attribute__((ext_vector_type(4))) short s16x4;
typedef __attribute__((ext_vector_type(8))) short s16x8;
typedef __attribute__((ext_vector_type(4))) unsigned u32x4;
typedef __attribute__((ext_vector_type(2))) unsigned u32x2;
#define LAS __attribute__((address_space(3)))
typedef LAS char* lptr;

template <class F, int... I> DI void sfor_impl(F&& f, std::integer_sequence<int, I...>) { (f(std::integral_constant<int, I>{}), ...); }
template <int N, class F> DI void sfor(F&& f) { sfor_impl(f, std::make_integer_sequence<int, N>{}); }

constexpr double kPi = 3.14159265358979323846264338327950288;
constexpr double c_cos(double x) { double x2 = x * x, term = 1.0, sum = 1.0; for (int i = 1; i < 18; ++i) { term *= -x2 / ((2 * i - 1) * (2 * i)); sum += term; } return sum; }
constexpr uint16_t c_f2bf(float f) { uint32_t u = __builtin_bit_cast(uint32_t, f); u += 0x7fffu + ((u >> 16) & 1u); return (uint16_t)(u >> 16); }
struct Cos512 { uint16_t v[512]; constexpr Cos512() : v{} { for (int m = 0; m < 512; ++m) { int mm = m <= 256 ? m : 512 - m; double c = c_cos(2.0 * kPi * mm / 512.0); if (mm == 128) c = 0.0; v[m] = c_f2bf((float)c); } } };
constexpr Cos512 C512{};
constexpr uint16_t fval(int d, int c) { const int T = c >> 4, j = 16 * (T >> 1) + (c & 15); const int m = (T & 1) == 0 ? ((d * j) & 511) : (j == 0 ? (d & 1) * 256 : ((d * j + 128) & 511)); return C512.v[m]; }

template <int T0> struct FPart { uint16_t v[2 * 8192];
  constexpr FPart() : v{} { for (int t = 0; t < 2; ++t) for (int ks = 0; ks < 16; ++ks) for (int l = 0; l < 64; ++l) for (int e = 0; e < 8; ++e)
    v[((t * 16 + ks) * 64 + l) * 8 + e] = fval(32 * ks + 8 * (l >> 4) + e, 16 * (T0 + t) + (l & 15)); } };
template <int P> struct GPart { uint16_t v[16384];
  constexpr GPart() : v{} { for (int s = 0; s < 4; ++s) for (int dt = 0; dt < 4; ++dt) for (int ksl = 0; ksl < 2; ++ksl) for (int l = 0; l < 64; ++l) for (int e = 0; e < 8; ++e)
    v[(((s * 4 + dt) * 2 + ksl) * 64 + l) * 8 + e] = fval(64 * (P >> 1) + 16 * dt + (l & 15), 32 * (2 * (4 * (P & 1) + s) + ksl) + 16 * (e >> 2) + 4 * (l >> 4) + (e & 3)); } };
struct FFTC { uint16_t t1[4 * 512], t2[16 * 512], t3[16 * 512], t4[4 * 512];
  constexpr FFTC() : t1{}, t2{}, t3{}, t4{} {
    for (int l = 0; l < 64; ++l) for (int e = 0; e < 8; ++e) { const int g = l >> 4, li = l & 15, sl = 16 * (e >> 2) + 4 * g + (e & 3);
      for (int part = 0; part < 2; ++part) {
        for (int t = 0; t < 2; ++t) t1[((part * 2 + t) * 64 + l) * 8 + e] = C512.v[(16 * sl * (16 * t + li) + 128 * part) & 511];
        for (int b = 0; b < 2; ++b) for (int t = 0; t < 2; ++t) for (int s = 0; s < 2; ++s)
          t2[((((b * 2 + t) * 2 + s) * 2 + part) * 64 + l) * 8 + e] = C512.v[(8 * (32 * b + 16 * t + li) * (32 * s + 8 * g + e) + 128 * part) & 511];
        for (int b = 0; b < 2; ++b) for (int t = 0; t < 4; ++t)
          t3[(((b * 4 + t) * 2 + part) * 64 + l) * 8 + e] = C512.v[(8 * (16 * t + li) * (32 * b + sl) + 384 * part) & 511];
        for (int t = 0; t < 2; ++t) t4[((t * 2 + part) * 64 + l) * 8 + e] = C512.v[(16 * (8 * (li >> 2) + 4 * t + (li & 3)) * (8 * g + e) + 384 * part) & 511];
      } } } };
struct FBig { FPart<0> a0; FPart<2> a1; FPart<4> a2; FPart<6> a3; FPart<8> a4; FPart<10> a5; FPart<12> a6; FPart<14> a7; FPart<16> a8; FPart<18> a9; FPart<20> a10; FPart<22> a11; FPart<24> a12; FPart<26> a13; FPart<28> a14; FPart<30> a15; };
struct GBig { GPart<0> a0; GPart<1> a1; GPart<2> a2; GPart<3> a3; GPart<4> a4; GPart<5> a5; GPart<6> a6; GPart<7> a7; GPart<8> a8; GPart<9> a9; GPart<10> a10; GPart<11> a11; GPart<12> a12; GPart<13> a13; GPart<14> a14; GPart<15> a15; };
#ifdef __HIP_DEVICE_COMPILE__
#define FP(n) constexpr FPart<n> fp##n{};
FP(0) FP(2) FP(4) FP(6) FP(8) FP(10) FP(12) FP(14) FP(16) FP(18) FP(20) FP(22) FP(24) FP(26) FP(28) FP(30)
#define GP(n) constexpr GPart<n> gp##n{};
GP(0) GP(1) GP(2) GP(3) GP(4) GP(5) GP(6) GP(7) GP(8) GP(9) GP(10) GP(11) GP(12) GP(13) GP(14) GP(15)
__device__ const FBig d_FT = {fp0, fp2, fp4, fp6, fp8, fp10, fp12, fp14, fp16, fp18, fp20, fp22, fp24, fp26, fp28, fp30};
__device__ const GBig d_GT = {gp0, gp1, gp2, gp3, gp4, gp5, gp6, gp7, gp8, gp9, gp10, gp11, gp12, gp13, gp14, gp15};
__device__ const FFTC d_FC = FFTC();
#else
__device__ const uint16_t d_FT[16 * 16384] = {0};
__device__ const uint16_t d_GT[16 * 16384] = {0};
__device__ const uint16_t d_FC[40 * 512] = {0};
#endif

DI f32x4 mfma(bf16x8 a, bf16x8 b, f32x4 c) { return __builtin_amdgcn_mfma_f32_16x16x32_bf16(a, b, c, 0, 0, 0); }
DI bf16x8 lds_b128(lptr p) { return *(LAS bf16x8*)p; }
DI s16x4 lds_tr(lptr p) { return __builtin_amdgcn_ds_read_tr16_b64_v4i16((LAS s16x4*)p); }
DI bf16x8 lds_tr2(lptr p0, lptr p1) { s16x4 a = lds_tr(p0), b = lds_tr(p1); s16x8 v = __builtin_shufflevector(a, b, 0, 1, 2, 3, 4, 5, 6, 7); return __builtin_bit_cast(bf16x8, v); }
DI void glds16(const void* g, lptr l) { __builtin_amdgcn_global_load_lds((const unsigned*)g, (LAS unsigned*)l, 16, 0, 0); }
template <int N> DI void wait_vm() { asm volatile("s_waitcnt vmcnt(%0)" ::"n"(N) : "memory"); }
DI void wait_lgkm0() { asm volatile("s_waitcnt lgkmcnt(0)" ::: "memory"); }
DI void bar() { __builtin_amdgcn_s_barrier(); }
DI float dot2(unsigned a, unsigned b, float c) { return __builtin_amdgcn_fdot2_f32_bf16(__builtin_bit_cast(bf16x2, a), __builtin_bit_cast(bf16x2, b), c, false); }
DI float bf_round(float f) { return (float)(__bf16)f; }
DI float bf2f(uint16_t u) { return __builtin_bit_cast(float, (unsigned)u << 16); }
DI float bflo(unsigned u) { return __builtin_bit_cast(float, u << 16); }
DI float bfhi(unsigned u) { return __builtin_bit_cast(float, u & 0xffff0000u); }
DI bf16x8 pack8(f32x4 a, f32x4 b) { bf16x8 r; r[0] = (__bf16)a[0]; r[1] = (__bf16)a[1]; r[2] = (__bf16)a[2]; r[3] = (__bf16)a[3]; r[4] = (__bf16)b[0]; r[5] = (__bf16)b[1]; r[6] = (__bf16)b[2]; r[7] = (__bf16)b[3]; return r; }
DI bf16x4 pack4(f32x4 a) { bf16x4 r; r[0] = (__bf16)a[0]; r[1] = (__bf16)a[1]; r[2] = (__bf16)a[2]; r[3] = (__bf16)a[3]; return r; }
DI bf16x8 neg8(bf16x8 a) { u32x4 u = __builtin_bit_cast(u32x4, a); u ^= 0x80008000u; return __builtin_bit_cast(bf16x8, u); }

constexpr int S_ = 2048, D_ = 512, DFF = 1792;
constexpr size_t WS_H = 0;
constexpr size_t WS_AT = WS_H + (size_t)S_ * DFF * 2;
constexpr size_t WS_BT = WS_AT + (size_t)512 * S_ * 2;
constexpr size_t WS_WT = WS_BT + (size_t)512 * S_ * 2;

constexpr int K1_SLOT = 34816, K1_NS = 4, K1_STATS = K1_NS * K1_SLOT, K1_LDS = K1_STATS + 3 * 512;

DI void k1_issue(const char* xg, const char* wg, const char* fg, lptr slot, int lw, int lane, bool typeC, int st) {
#pragma unroll
  for (int i = 0; i < 4; ++i) glds16(xg + (size_t)i * 524288 + st * 128, slot + (32 * lw + 8 * i) * 128);
  if (!typeC) {
#pragma unroll
    for (int i = 0; i < 4; ++i) glds16(wg + (size_t)(64 * st + 4 * (i >> 1)) * 3584 + 128 * (i & 1), slot + 16384 + 1024 * (4 * lw + i));
  } else {
#pragma unroll
    for (int i = 0; i < 4; ++i) glds16(wg + (size_t)((i >> 1) * 16 + 2 * st + (i & 1)) * 1024, slot + 16384 + 1024 * (4 * lw + i));
  }
  if (lane < 32) glds16(fg + st * 2048, slot + 32768 + 512 * lw);
}

template <bool TYPEC>
DI void k1_loader(lptr lds, const char* x, const char* w1, int rb, int cg, int lw, int lane) {
  const int g = lane >> 4;
  const char* xg = x + (size_t)(64 * (lane >> 3) + 4 * rb + lw) * 1024 + (((lane & 7) ^ (lane >> 3)) * 16);
  const char* ft = (const char*)&d_FT;
  const int T0 = TYPEC ? 14 + 9 * (cg - 14) : cg;
  const char* wg = TYPEC ? ft + (size_t)(T0 + 2 * lw) * 16384 + lane * 16
                         : w1 + (size_t)(16 * lw + 8 * ((lane >> 3) & 1) + ((lane >> 1) & 3)) * 3584 + cg * 256 + (lane >> 4) * 32 + (lane & 1) * 16;
  const char* fg = ft + (size_t)(TYPEC ? T0 + 8 : T0) * 16384 + 512 * lw + lane * 16;
  sfor<4>([&](auto I) { k1_issue(xg, wg, fg, lds + I * K1_SLOT, lw, lane, TYPEC, I); });
  const int row = 32 * lw + (lane & 15);
  const int xo0 = row * 128 + ((g ^ (row & 7)) * 16), xo1 = row * 128 + (((g ^ (row & 7)) ^ 4) * 16);
  const int wo = 16384 + 4096 * (g >> 1) + 32 * (4 * (g & 1) + ((lane & 15) >> 2)) + 16 * ((lane & 3) >> 1) + 8 * (lane & 1) + 512 * lw;
  float sum[2] = {0.f, 0.f}, sq[2] = {0.f, 0.f}, cs[2] = {0.f, 0.f};
  sfor<8>([&](auto I) {
    constexpr int s = I;
    wait_vm<(s < 6 ? (s == 0 ? 27 : 18) : (s == 6 ? 9 : 0))>();
    wait_lgkm0();
    bar();
    if constexpr (s >= 1 && s + 3 < 8) k1_issue(xg, wg, fg, lds + ((s + 3) & 3) * K1_SLOT, lw, lane, TYPEC, s + 3);
    lptr sl = lds + (s & 3) * K1_SLOT;
#pragma unroll
    for (int m = 0; m < 2; ++m) {
      u32x4 a = __builtin_bit_cast(u32x4, lds_b128(sl + xo0 + m * 2048)), b = __builtin_bit_cast(u32x4, lds_b128(sl + xo1 + m * 2048));
#pragma unroll
      for (int e = 0; e < 4; ++e) { sum[m] = dot2(a[e], 0x3f803f80u, sum[m]); sq[m] = dot2(a[e], a[e], sq[m]); }
#pragma unroll
      for (int e = 0; e < 4; ++e) { sum[m] = dot2(b[e], 0x3f803f80u, sum[m]); sq[m] = dot2(b[e], b[e], sq[m]); }
    }
    if constexpr (!TYPEC) {
#pragma unroll
      for (int t = 0; t < 2; ++t)
#pragma unroll
        for (int ks = 0; ks < 2; ++ks) {
          u32x4 a = __builtin_bit_cast(u32x4, lds_tr2(sl + wo + 256 * t + 8192 * ks, sl + wo + 256 * t + 8192 * ks + 2048));
#pragma unroll
          for (int e = 0; e < 4; ++e) cs[t] = dot2(a[e], 0x3f803f80u, cs[t]);
        }
    }
  });
  LAS float* st = (LAS float*)(lds + K1_STATS);
#pragma unroll
  for (int m = 0; m < 2; ++m) {
    float s1 = sum[m], s2 = sq[m];
    s1 += __shfl_xor(s1, 16); s1 += __shfl_xor(s1, 32); s2 += __shfl_xor(s2, 16); s2 += __shfl_xor(s2, 32);
    const float mean = s1 * (1.f / 512.f);
    const float var = fmaxf(s2 * (1.f / 512.f) - mean * mean, 0.f);
    const float den = bf_round(sqrtf(bf_round(bf_round(var) + 1e-5f)));
    if (lane < 16) { st[row + 16 * m] = bf_round(mean); st[128 + row + 16 * m] = 1.f / den; }
  }
  if constexpr (!TYPEC) {
#pragma unroll
    for (int t = 0; t < 2; ++t) { float c = cs[t]; c += __shfl_xor(c, 16); c += __shfl_xor(c, 32); if (lane < 16) st[256 + 16 * (2 * lw + t) + lane] = c; }
  }
  wait_lgkm0();
  bar();
}

DI void k1_wprep(const uint16_t* wr, const uint16_t* wi, uint16_t* WT, int id, int w, int lane) {
  const int quad = id & 7, b = (id >> 3) & 1, g = (id >> 4) & 3, cq = id >> 6;
  const int k1 = 4 * quad + (lane >> 4);
  const int npass = (cq == 0 && w == 0) ? 2 : 1;
  for (int p = 0; p < npass; ++p) {
    const int j = p == 0 ? 64 * cq + 16 * w + (lane & 15) : 256;
    unsigned v[8];
#pragma unroll
    for (int e = 0; e < 8; ++e) { const int k = k1 + 32 * (32 * b + 16 * (e >> 2) + 4 * g + (e & 3)); v[e] = (unsigned)wr[k * 257 + j] | ((unsigned)wi[k * 257 + j] << 16); }
    if (p == 0 || (lane & 15) == 0) {
      u32x4* dst = (u32x4*)(WT + (size_t)j * 4096 + (((k1 >> 4) + 2 * b) * 64 + (k1 & 15) + 16 * g) * 16);
      dst[0] = u32x4{v[0], v[1], v[2], v[3]}; dst[1] = u32x4{v[4], v[5], v[6], v[7]};
    }
  }
}

template <bool TYPEC>
DI void k1_consumer(lptr lds, char* ws, int rb, int cg, int w, int lane) {
  const int g = lane >> 4, li = lane & 15;
  const int row = 32 * w + li;
  const int xo[2] = {row * 128 + ((g ^ (row & 7)) * 16), row * 128 + (((g ^ (row & 7)) ^ 4) * 16)};
  const int wo = TYPEC ? 16384 + lane * 16 : 16384 + 4096 * (g >> 1) + 32 * (4 * (g & 1) + (li >> 2)) + 16 * ((lane & 3) >> 1) + 8 * (lane & 1);
  const int fo = 32768 + lane * 16;
  f32x4 acc[2][9];
#pragma unroll
  for (int m = 0; m < 2; ++m)
#pragma unroll
    for (int t = 0; t < 9; ++t) acc[m][t] = f32x4{0.f, 0.f, 0.f, 0.f};
  bf16x8 xa[2][2], wf[2][9];
  auto rd = [&](int buf, lptr sl, int ks) {
    xa[buf][0] = lds_b128(sl + xo[ks]); xa[buf][1] = lds_b128(sl + xo[ks] + 2048);
#pragma unroll
    for (int t = 0; t < 8; ++t) {
      if (TYPEC) wf[buf][t] = lds_b128(sl + wo + t * 2048 + ks * 1024);
      else wf[buf][t] = lds_tr2(sl + wo + 256 * t + 8192 * ks, sl + wo + 256 * t + 8192 * ks + 2048);
    }
    wf[buf][8] = lds_b128(sl + fo + ks * 1024);
  };
  auto mm = [&](int buf) {
#pragma unroll
    for (int t = 0; t < 9; ++t)
#pragma unroll
      for (int m = 0; m < 2; ++m) {
        if (TYPEC || t == 8) acc[m][t] = mfma(xa[buf][m], wf[buf][t], acc[m][t]);
        else acc[m][t] = mfma(wf[buf][t], xa[buf][m], acc[m][t]);
      }
  };
  bar();
  rd(0, lds, 0);
  sfor<8>([&](auto I) {
    constexpr int s = I;
    rd(1, lds + (s & 3) * K1_SLOT, 1);
    mm(0);
    if constexpr (s < 7) { wait_lgkm0(); bar(); rd(0, lds + ((s + 1) & 3) * K1_SLOT, 0); }
    mm(1);
  });
  bar();
  LAS float* st = (LAS float*)(lds + K1_STATS);
  const int T0 = TYPEC ? 14 + 9 * (cg - 14) : cg;
  uint16_t* AT = (uint16_t*)(ws + WS_AT);
  {
    f32x4 mm4[2], rr4[2];
#pragma unroll
    for (int m = 0; m < 2; ++m) { mm4[m] = *(LAS f32x4*)(st + 32 * w + 16 * m + 4 * g); rr4[m] = *(LAS f32x4*)(st + 128 + 32 * w + 16 * m + 4 * g); }
#pragma unroll
    for (int t = (TYPEC ? 0 : 8); t < 9; ++t) {
      const int T = TYPEC ? T0 + t : T0;
      const float csf = (T == 0 && li == 0) ? 512.f : 0.f;
      f32x4 v0 = (acc[0][t] - mm4[0] * csf) * rr4[0], v1 = (acc[1][t] - mm4[1] * csf) * rr4[1];
      *(bf16x8*)(AT + (size_t)(16 * T + li) * 2048 + 32 * (4 * rb + w) + 8 * g) = pack8(v0, v1);
    }
  }
  if constexpr (!TYPEC) {
    uint16_t* H = (uint16_t*)(ws + WS_H);
#pragma unroll
    for (int m = 0; m < 2; ++m) {
      const float mu = st[32 * w + 16 * m + li], rs = st[128 + 32 * w + 16 * m + li];
#pragma unroll
      for (int u = 0; u < 4; ++u) {
        const f32x4 c0 = *(LAS f32x4*)(st + 256 + 32 * u + 4 * g), c1 = *(LAS f32x4*)(st + 256 + 32 * u + 16 + 4 * g);
        f32x4 v0 = (acc[m][2 * u] - c0 * mu) * rs, v1 = (acc[m][2 * u + 1] - c1 * mu) * rs;
#pragma unroll
        for (int e = 0; e < 4; ++e) { v0[e] = fmaxf(v0[e], 0.f); v1[e] = fmaxf(v1[e], 0.f); }
        *(bf16x8*)(H + ((size_t)((8 * rb + 2 * w + m) * 56 + 4 * cg + u) * 64 + lane) * 8) = pack8(v0, v1);
      }
    }
  }
}

__global__ __launch_bounds__(512) void k1_gemm1(const uint16_t* __restrict__ x, const uint16_t* __restrict__ wr, const uint16_t* __restrict__ wi,
                                                const uint16_t* __restrict__ w1, char* __restrict__ ws) {
  extern __shared__ __attribute__((aligned(16))) char smem[];
  lptr lds = (lptr)smem;
  const int wid = __builtin_amdgcn_readfirstlane(threadIdx.x >> 6), lane = threadIdx.x & 63;
  const int id = blockIdx.x, xcd = id & 7, loc = id >> 3;
  const int rb = 8 * (xcd & 1) + (loc & 7), cg = 4 * (xcd >> 1) + (loc >> 3);
  if (wid >= 4) {
    if (cg >= 14) k1_loader<true>(lds, (const char*)x, (const char*)w1, rb, cg, wid - 4, lane);
    else k1_loader<false>(lds, (const char*)x, (const char*)w1, rb, cg, wid - 4, lane);
  } else {
    k1_wprep(wr, wi, (uint16_t*)(ws + WS_WT), id, wid, lane);
    if (cg >= 14) k1_consumer<true>(lds, ws, rb, cg, wid, lane);
    else k1_consumer<false>(lds, ws, rb, cg, wid, lane);
  }
}

constexpr int K2_Y = 0, K2_V = 9216, K2_LDS = K2_V + 36864;

__global__ __launch_bounds__(256) void k2_fft(char* __restrict__ ws) {
  extern __shared__ __attribute__((aligned(16))) char smem[];
  lptr lds = (lptr)smem;
  const int w = __builtin_amdgcn_readfirstlane(threadIdx.x >> 6), lane = threadIdx.x & 63, g = lane >> 4, li = lane & 15;
  const int j = blockIdx.x, a = w & 1, b = w >> 1;
  const int cre = 32 * (j >> 4) + (j & 15), cim = cre + 16;
  const uint16_t* AT = (const uint16_t*)(ws + WS_AT);
  const uint16_t* WT = (const uint16_t*)(ws + WS_WT);
  uint16_t* BT = (uint16_t*)(ws + WS_BT);
  const bf16x8 zr = *(const bf16x8*)(AT + (size_t)cre * 2048 + 32 * (16 * w + li) + 8 * g);
  const bf16x8 zi = *(const bf16x8*)(AT + (size_t)cim * 2048 + 32 * (16 * w + li) + 8 * g);
  const u32x4* wp = (const u32x4*)(WT + (size_t)j * 4096 + (w * 64 + lane) * 16);
  const u32x4 wv0 = wp[0], wv1 = wp[1];
  const bf16x8* fc = (const bf16x8*)&d_FC;
  bf16x8 c1[2][2], c2[2][2][2], c3[4][2], c4[2][2];
#pragma unroll
  for (int p = 0; p < 2; ++p)
#pragma unroll
    for (int t = 0; t < 2; ++t) { c1[p][t] = fc[(p * 2 + t) * 64 + lane]; c4[t][p] = fc[(36 + t * 2 + p) * 64 + lane]; }
#pragma unroll
  for (int t = 0; t < 2; ++t)
#pragma unroll
    for (int s = 0; s < 2; ++s)
#pragma unroll
      for (int p = 0; p < 2; ++p) c2[t][s][p] = fc[(4 + ((b * 2 + t) * 2 + s) * 2 + p) * 64 + lane];
#pragma unroll
  for (int t = 0; t < 4; ++t)
#pragma unroll
    for (int p = 0; p < 2; ++p) c3[t][p] = fc[(20 + (b * 4 + t) * 2 + p) * 64 + lane];
  f32x4 t1c[2], t1s[2]; float t2c[8], t2s[8];
#pragma unroll
  for (int t = 0; t < 2; ++t)
#pragma unroll
    for (int r = 0; r < 4; ++r) { const float f = (float)((16 * w + 4 * g + r) * (16 * t + li)) * (1.f / 2048.f); t1c[t][r] = __builtin_amdgcn_cosf(f); t1s[t][r] = __builtin_amdgcn_sinf(f); }
#pragma unroll
  for (int e = 0; e < 8; ++e) { const float f = (float)((16 * w + li) * (8 * g + e)) * (1.f / 2048.f); t2c[e] = __builtin_amdgcn_cosf(f); t2s[e] = __builtin_amdgcn_sinf(f); }
  const f32x4 z4 = {0.f, 0.f, 0.f, 0.f};
  {
    const bf16x8 nzi = neg8(zi);
#pragma unroll
    for (int t = 0; t < 2; ++t) {
      f32x4 yr = mfma(zr, c1[0][t], z4); yr = mfma(nzi, c1[1][t], yr);
      f32x4 yi = mfma(zr, c1[1][t], z4); yi = mfma(zi, c1[0][t], yi);
      const f32x4 pr = yr * t1c[t] + yi * t1s[t], pi = yi * t1c[t] - yr * t1s[t];
      lptr p = lds + K2_Y + (16 * t + li) * 144 + (16 * w + 4 * g) * 2;
      *(LAS bf16x4*)p = pack4(pr); *(LAS bf16x4*)(p + 4608) = pack4(pi);
    }
  }
  __syncthreads();
  f32x4 xr[2], xi[2];
  {
    bf16x8 yr[2], yi[2], nyi[2];
#pragma unroll
    for (int s = 0; s < 2; ++s) { lptr p = lds + K2_Y + (16 * a + li) * 144 + (32 * s + 8 * g) * 2; yr[s] = lds_b128(p); yi[s] = lds_b128(p + 4608); nyi[s] = neg8(yi[s]); }
#pragma unroll
    for (int t = 0; t < 2; ++t) {
      xr[t] = z4; xi[t] = z4;
#pragma unroll
      for (int s = 0; s < 2; ++s) {
        xr[t] = mfma(c2[t][s][0], yr[s], xr[t]); xr[t] = mfma(c2[t][s][1], nyi[s], xr[t]);
        xi[t] = mfma(c2[t][s][1], yr[s], xi[t]); xi[t] = mfma(c2[t][s][0], yi[s], xi[t]);
      }
    }
  }
  f32x4 pr[2], pi[2];
  if (j != 0) {
#pragma unroll
    for (int t = 0; t < 2; ++t)
#pragma unroll
      for (int r = 0; r < 4; ++r) {
        const unsigned wv = t == 0 ? wv0[r] : wv1[r];
        const float wre = bflo(wv), wim = bfhi(wv);
        pr[t][r] = xr[t][r] * wre - xi[t][r] * wim; pi[t][r] = xr[t][r] * wim + xi[t][r] * wre;
      }
  } else {
    const u32x4* wq = (const u32x4*)(WT + (size_t)256 * 4096 + (w * 64 + lane) * 16);
    const u32x4 wn0 = wq[0], wn1 = wq[1];
    LAS f32x2* X = (LAS f32x2*)(lds + K2_V);
    LAS unsigned* W0 = (LAS unsigned*)(lds + K2_V + 16384);
    LAS unsigned* WN = (LAS unsigned*)(lds + K2_V + 24576);
#pragma unroll
    for (int t = 0; t < 2; ++t)
#pragma unroll
      for (int r = 0; r < 4; ++r) { const int k = 16 * a + li + 32 * (32 * b + 16 * t + 4 * g + r); X[k] = f32x2{xr[t][r], xi[t][r]}; W0[k] = t == 0 ? wv0[r] : wv1[r]; WN[k] = t == 0 ? wn0[r] : wn1[r]; }
    __syncthreads();
#pragma unroll
    for (int t = 0; t < 2; ++t)
#pragma unroll
      for (int r = 0; r < 4; ++r) {
        const int k = 16 * a + li + 32 * (32 * b + 16 * t + 4 * g + r), km = (2048 - k) & 2047;
        const f32x2 xm = X[km]; const unsigned w0 = W0[k], w0m = W0[km], wn = WN[k], wnm = WN[km];
        const float ar = 0.5f * (bflo(w0) + bflo(w0m)), ai = 0.5f * (bfhi(w0) - bfhi(w0m));
        const float br = 0.5f * (bflo(wn) + bflo(wnm)), bi = 0.5f * (bfhi(wn) - bfhi(wnm));
        const float ur = 0.25f * (ar + br), ui = 0.25f * (ai + bi), vr = 0.25f * (ar - br), vi = 0.25f * (ai - bi);
        pr[t][r] = xr[t][r] * ur - xi[t][r] * ui + xm[0] * vr + xm[1] * vi;
        pi[t][r] = xr[t][r] * ui + xi[t][r] * ur + xm[0] * vi - xm[1] * vr;
      }
    __syncthreads();
  }
  {
    const bf16x8 Pr = pack8(pr[0], pr[1]), Pi = pack8(pi[0], pi[1]), nPi = neg8(Pi);
#pragma unroll
    for (int t = 0; t < 4; ++t) {
      f32x4 vr = mfma(Pr, c3[t][0], z4); vr = mfma(nPi, c3[t][1], vr);
      f32x4 vi = mfma(Pr, c3[t][1], z4); vi = mfma(Pi, c3[t][0], vi);
      lptr p = lds + K2_V + b * 18432 + (16 * t + li) * 144 + (16 * a + 4 * g) * 4;
      *(LAS f32x4*)p = vr; *(LAS f32x4*)(p + 9216) = vi;
    }
  }
  __syncthreads();
  {
    lptr p = lds + K2_V + (16 * w + li) * 144 + 8 * g * 4;
    f32x4 vr[2], vi[2], ur[2], ui[2];
#pragma unroll
    for (int h = 0; h < 2; ++h) {
      vr[h] = *(LAS f32x4*)(p + 16 * h) + *(LAS f32x4*)(p + 18432 + 16 * h);
      vi[h] = *(LAS f32x4*)(p + 9216 + 16 * h) + *(LAS f32x4*)(p + 18432 + 9216 + 16 * h);
#pragma unroll
      for (int r = 0; r < 4; ++r) { const float c = t2c[4 * h + r], s = t2s[4 * h + r]; ur[h][r] = vr[h][r] * c - vi[h][r] * s; ui[h][r] = vr[h][r] * s + vi[h][r] * c; }
    }
    const bf16x8 Vr = pack8(ur[0], ur[1]), Vi = pack8(ui[0], ui[1]), nVi = neg8(Vi);
    f32x4 yr[2], yi[2];
#pragma unroll
    for (int t = 0; t < 2; ++t) {
      yr[t] = mfma(c4[t][0], Vr, z4); yr[t] = mfma(c4[t][1], nVi, yr[t]);
      yi[t] = mfma(c4[t][1], Vr, z4); yi[t] = mfma(c4[t][0], Vi, yi[t]);
    }
    *(bf16x8*)(BT + (size_t)cre * 2048 + 32 * (16 * w + li) + 8 * g) = pack8(yr[0], yr[1]);
    *(bf16x8*)(BT + (size_t)cim * 2048 + 32 * (16 * w + li) + 8 * g) = pack8(yi[0], yi[1]);
  }
}

constexpr int K3_NS = 18, K3_LDS = K3_NS * 8192, K3_PD = 16;
constexpr int k3_first(int step) { return step < 28 ? step : 28 + 2 * (step - 28); }
constexpr int k3_end(int step) { return step < 28 ? step + 1 : 30 + 2 * (step - 28); }
constexpr int k3_issued(int step) {
  int n = K3_NS; if (step >= 2) n += k3_end(step - 2) - 0; return n > 44 ? 44 : n; }

__global__ __launch_bounds__(512) void k3_out(const uint16_t* __restrict__ x, const uint16_t* __restrict__ w2, const char* __restrict__ ws, uint16_t* __restrict__ out) {
  extern __shared__ __attribute__((aligned(16))) char smem[];
  lptr lds = (lptr)smem;
  const int wid = __builtin_amdgcn_readfirstlane(threadIdx.x >> 6), lane = threadIdx.x & 63, g = lane >> 4, li = lane & 15;
  const int id = blockIdx.x, xcd = id & 7, loc = id >> 3;
  const int rowblk = 4 * xcd + (loc & 3), cb = loc >> 2;
  if (wid >= 4) {
    const int lw = wid - 4;
    const int lo = (lane >> 4) * 32 + (lane & 1) * 16, lr = 16 * lw + ((lane >> 1) & 7);
    const char* wg = (const char*)w2 + (size_t)lr * 1024 + cb * 128 + lo;
    const char* bg = ws + WS_BT + (size_t)lr * 4096 + rowblk * 128 + lo;
    const char* gg = (const char*)&d_GT + (size_t)cb * 65536 + 2048 * lw + lane * 16;
    auto issue = [&](auto Q) {
      constexpr int q = Q;
      lptr sl = lds + (q % K3_NS) * 8192 + 2048 * lw;
      if constexpr (q < 28) { glds16(wg + (size_t)q * 65536, sl); glds16(wg + (size_t)q * 65536 + 8192, sl + 1024); }
      else if constexpr (((q - 28) & 1) == 0) { glds16(gg + ((q - 28) >> 1) * 8192, sl); glds16(gg + ((q - 28) >> 1) * 8192 + 1024, sl + 1024); }
      else { glds16(bg + (size_t)((q - 28) >> 1) * 262144, sl); glds16(bg + (size_t)((q - 28) >> 1) * 262144 + 32768, sl + 1024); }
    };
    sfor<K3_NS>(issue);
    sfor<36>([&](auto I) {
      constexpr int s = I;
      constexpr int issued = (s >= 2 ? K3_NS + k3_end(s - 2) : K3_NS) > 44 ? 44 : (s >= 2 ? K3_NS + k3_end(s - 2) : K3_NS);
      wait_vm<2 * (issued - k3_end(s))>();
      bar();
      if constexpr (s >= 1) {
        constexpr int lo_ = K3_NS + k3_first(s - 1), hi_ = K3_NS + k3_end(s - 1);
        if constexpr (lo_ < 44) issue(std::integral_constant<int, lo_>{});
        if constexpr (lo_ + 1 < hi_ && lo_ + 1 < 44) issue(std::integral_constant<int, lo_ + 1>{});
      }
    });
    return;
  }
  const int w = wid;
  const int rt = 4 * rowblk + w;
  const int n = 64 * (16 * (rt & 1) + li) + (rt >> 1);
  const bf16x8* hp = (const bf16x8*)(ws + WS_H) + (size_t)rt * 56 * 64 + lane;
  bf16x8 hr[K3_PD];
#pragma unroll
  for (int i = 0; i < K3_PD; ++i) hr[i] = hp[i * 64];
  u32x2 xv[4];
#pragma unroll
  for (int t = 0; t < 4; ++t) xv[t] = *(const u32x2*)(x + (size_t)n * 512 + 64 * cb + 16 * t + 4 * g);
  const int tb = 1024 * (g >> 1) + 32 * (4 * (g & 1) + (li >> 2)) + 16 * ((lane & 3) >> 1) + 8 * (lane & 1);
  f32x4 acc1[4], acc2[4];
#pragma unroll
  for (int t = 0; t < 4; ++t) { acc1[t] = f32x4{0.f, 0.f, 0.f, 0.f}; acc2[t] = acc1[t]; }
  bf16x8 fr[2][10];
  auto rd = [&](auto I) {
    constexpr int s = I, buf = s & 1;
    if constexpr (s < 28) {
      lptr sl = lds + (s % K3_NS) * 8192 + tb;
#pragma unroll
      for (int ksl = 0; ksl < 2; ++ksl)
#pragma unroll
        for (int t = 0; t < 4; ++t) fr[buf][ksl * 4 + t] = lds_tr2(sl + 4096 * ksl + 256 * t, sl + 4096 * ksl + 2048 + 256 * t);
    } else {
      lptr sg = lds + (k3_first(s) % K3_NS) * 8192 + lane * 16, sb = lds + ((k3_first(s) + 1) % K3_NS) * 8192 + tb + 256 * w;
#pragma unroll
      for (int ksl = 0; ksl < 2; ++ksl) {
#pragma unroll
        for (int t = 0; t < 4; ++t) fr[buf][ksl * 4 + t] = lds_b128(sg + t * 2048 + ksl * 1024);
        fr[buf][8 + ksl] = lds_tr2(sb + 4096 * ksl, sb + 4096 * ksl + 2048);
      }
    }
  };
  bar();
  rd(std::integral_constant<int, 0>{});
  sfor<36>([&](auto I) {
    constexpr int s = I, buf = s & 1;
    if constexpr (s + 1 < 36) { wait_lgkm0(); bar(); rd(std::integral_constant<int, s + 1>{}); }
    if constexpr (s < 28) {
#pragma unroll
      for (int ksl = 0; ksl < 2; ++ksl) {
        constexpr int dummy = 0; (void)dummy;
        const int ks = 2 * s + ksl;
        const bf16x8 hf = hr[ks % K3_PD];
#pragma unroll
        for (int t = 0; t < 4; ++t) acc1[t] = mfma(fr[buf][ksl * 4 + t], hf, acc1[t]);
        if (ks + K3_PD < 56) hr[ks % K3_PD] = hp[(ks + K3_PD) * 64];
      }
    } else {
#pragma unroll
      for (int ksl = 0; ksl < 2; ++ksl)
#pragma unroll
        for (int t = 0; t < 4; ++t) acc2[t] = mfma(fr[buf][ksl * 4 + t], fr[buf][8 + ksl], acc2[t]);
    }
  });
#pragma unroll
  for (int t = 0; t < 4; ++t) {
    float o[4];
#pragma unroll
    for (int r = 0; r < 4; ++r) {
      const float v = bf_round(acc1[t][r]);
      const float gate = bf_round(v / (1.f + __expf(-v)));
      const float sv = bf_round(acc2[t][r] * (1.f / 524288.f));
      const float xin = (r & 1) ? bfhi(xv[t][r >> 1]) : bflo(xv[t][r >> 1]);
      o[r] = xin + bf_round(sv * gate);
    }
    *(bf16x4*)(out + (size_t)n * 512 + 64 * cb + 16 * t + 4 * g) = pack4(f32x4{o[0], o[1], o[2], o[3]});
  }
}

extern "C" void kernel_launch(void* const* d_in, const int* in_sizes, int n_in, void* d_out, int out_size, void* d_ws, size_t ws_size, hipStream_t stream) {
  const uint16_t* x = (const uint16_t*)d_in[0];
  const uint16_t* wr = (const uint16_t*)d_in[1];
  const uint16_t* wi = (const uint16_t*)d_in[2];
  const uint16_t* w1 = (const uint16_t*)d_in[4];
  const uint16_t* w2 = (const uint16_t*)d_in[5];
  char* ws = (char*)d_ws;
  hipFuncSetAttribute((const void*)k1_gemm1, hipFuncAttributeMaxDynamicSharedMemorySize, K1_LDS);
  hipFuncSetAttribute((const void*)k2_fft, hipFuncAttributeMaxDynamicSharedMemorySize, K2_LDS);
  hipFuncSetAttribute((const void*)k3_out, hipFuncAttributeMaxDynamicSharedMemorySize, K3_LDS);
  k1_gemm1<<<256, 512, K1_LDS, stream>>>(x, wr, wi, w1, ws);
  k2_fft<<<256, 256, K2_LDS, stream>>>(ws);
  k3_out<<<256, 512, K3_LDS, stream>>>(x, w2, ws, (uint16_t*)d_out);
}
```

```cpp
#include <hip/hip_runtime.h>
#include <stdint.h>
#include <utility>

#define DI __device__ __forceinline__
typedef __attribute__((ext_vector_type(8))) __bf16 bf16x8;
typedef __attribute__((ext_vector_type(4))) __bf16 bf16x4;
typedef __attribute__((ext_vector_type(2))) __bf16 bf16x2;
typedef __attribute__((ext_vector_type(4))) float f32x4;
typedef __attribute__((ext_vector_type(2))) float f32x2;
typedef __attribute__((ext_vector_type(4))) short s16x4;
typedef __attribute__((ext_vector_type(8))) short s16x8;
typedef __attribute__((ext_vector_type(4))) unsigned u32x4;
typedef __attribute__((ext_vector_type(2))) unsigned u32x2;
typedef unsigned long long u64;
#define LAS __attribute__((address_space(3)))
typedef LAS char* lptr;
typedef __amdgpu_buffer_rsrc_t rsrc_t;

template <class F, int... I> DI void sfor_impl(F&& f, std::integer_sequence<int, I...>) { (f(std::integral_constant<int, I>{}), ...); }
template <int N, class F> DI void sfor(F&& f) { sfor_impl(f, std::make_integer_sequence<int, N>{}); }

constexpr double kPi = 3.14159265358979323846264338327950288;
constexpr double c_cos(double x) { double x2 = x * x, term = 1.0, sum = 1.0; for (int i = 1; i < 18; ++i) { term *= -x2 / ((2 * i - 1) * (2 * i)); sum += term; } return sum; }
constexpr uint16_t c_f2bf(float f) { uint32_t u = __builtin_bit_cast(uint32_t, f); u += 0x7fffu + ((u >> 16) & 1u); return (uint16_t)(u >> 16); }
struct Cos2048 { float v[2048]; constexpr Cos2048() : v{} { for (int m = 0; m < 2048; ++m) { int mm = m <= 1024 ? m : 2048 - m; double c = c_cos(2.0 * kPi * mm / 2048.0); if (mm == 512) c = 0.0; v[m] = (float)c; } } };
constexpr Cos2048 C2K{};
struct Cos512 { uint16_t v[512]; constexpr Cos512() : v{} { for (int m = 0; m < 512; ++m) v[m] = c_f2bf(C2K.v[4 * m]); } };
constexpr Cos512 C512{};
constexpr uint16_t fval(int d, int c) { const int T = c >> 4, j = 16 * (T >> 1) + (c & 15); const int m = (T & 1) == 0 ? ((d * j) & 511) : (j == 0 ? (d & 1) * 256 : ((d * j + 128) & 511)); return C512.v[m]; }
constexpr int c_sl(int g, int e) { return 16 * (e >> 2) + 4 * g + (e & 3); }
constexpr int c_rho(int v, int t, int li) { return 32 * v + 8 * (li >> 2) + 4 * t + (li & 3); }

template <int CG> struct FPart { uint16_t v[16384];
  constexpr FPart() : v{} { for (int st = 0; st < 4; ++st) for (int t = 0; t < 2; ++t) for (int k = 0; k < 4; ++k) for (int l = 0; l < 64; ++l) for (int e = 0; e < 8; ++e)
    v[(((st * 2 + t) * 4 + k) * 64 + l) * 8 + e] = fval(128 * st + 32 * k + 8 * (l >> 4) + e, 32 * CG + 16 * t + (l & 15)); } };
template <int P> struct GPart { uint16_t v[16384];
  constexpr GPart() : v{} { for (int s = 0; s < 4; ++s) for (int t = 0; t < 4; ++t) for (int k = 0; k < 2; ++k) for (int l = 0; l < 64; ++l) for (int e = 0; e < 8; ++e)
    v[(((s * 4 + t) * 2 + k) * 64 + l) * 8 + e] = fval(64 * (P >> 1) + 16 * t + (l & 15), 64 * (4 * (P & 1) + s) + 32 * k + 8 * (l >> 4) + e); } };
struct FFTC { uint16_t c64[8192]; float tw1[4096], tw3[4096]; uint16_t c32[4096];
  constexpr FFTC() : c64{}, tw1{}, tw3{}, c32{} {
    for (int l = 0; l < 64; ++l) { const int g = l >> 4, li = l & 15;
      for (int p = 0; p < 2; ++p) {
        for (int e = 0; e < 8; ++e) {
          for (int v = 0; v < 2; ++v) for (int t = 0; t < 2; ++t) for (int ks = 0; ks < 2; ++ks)
            c64[((((v * 2 + t) * 2 + ks) * 2 + p) * 64 + l) * 8 + e] = C512.v[(8 * c_rho(v, t, li) * (32 * ks + c_sl(g, e)) + 128 * p) & 511];
          for (int t = 0; t < 2; ++t) c32[((t * 2 + p) * 64 + l) * 8 + e] = C512.v[(16 * (16 * t + li) * c_sl(g, e) + 128 * p) & 511];
        }
        for (int w = 0; w < 4; ++w) for (int t = 0; t < 2; ++t) for (int r = 0; r < 4; ++r) {
          tw1[(((w * 2 + t) * 2 + p) * 64 + l) * 4 + r] = C2K.v[((16 * (w & 1) + 4 * g + r) * c_rho(w >> 1, t, li) + 1536 * p) & 2047];
          tw3[(((w * 2 + t) * 2 + p) * 64 + l) * 4 + r] = C2K.v[((16 * w + 4 * g + r) * (16 * t + li) + 1536 * p) & 2047];
        } } } } };
struct FBig { FPart<0> a0; FPart<1> a1; FPart<2> a2; FPart<3> a3; FPart<4> a4; FPart<5> a5; FPart<6> a6; FPart<7> a7; FPart<8> a8; FPart<9> a9; FPart<10> a10; FPart<11> a11; FPart<12> a12; FPart<13> a13; FPart<14> a14; FPart<15> a15; };
struct GBig { GPart<0> a0; GPart<1> a1; GPart<2> a2; GPart<3> a3; GPart<4> a4; GPart<5> a5; GPart<6> a6; GPart<7> a7; GPart<8> a8; GPart<9> a9; GPart<10> a10; GPart<11> a11; GPart<12> a12; GPart<13> a13; GPart<14> a14; GPart<15> a15; };
#ifdef __HIP_DEVICE_COMPILE__
#define FP(n) constexpr FPart<n> fp##n{};
FP(0) FP(1) FP(2) FP(3) FP(4) FP(5) FP(6) FP(7) FP(8) FP(9) FP(10) FP(11) FP(12) FP(13) FP(14) FP(15)
#define GP(n) constexpr GPart<n> gp##n{};
GP(0) GP(1) GP(2) GP(3) GP(4) GP(5) GP(6) GP(7) GP(8) GP(9) GP(10) GP(11) GP(12) GP(13) GP(14) GP(15)
__device__ const FBig d_FT = {fp0, fp1, fp2, fp3, fp4, fp5, fp6, fp7, fp8, fp9, fp10, fp11, fp12, fp13, fp14, fp15};
__device__ const GBig d_GT = {gp0, gp1, gp2, gp3, gp4, gp5, gp6, gp7, gp8, gp9, gp10, gp11, gp12, gp13, gp14, gp15};
__device__ const FFTC d_FC = FFTC();
#else
__device__ const uint16_t d_FT[16 * 16384] = {0};
__device__ const uint16_t d_GT[16 * 16384] = {0};
__device__ const uint16_t d_FC[28672] = {0};
#endif

DI f32x4 mfma(bf16x8 a, bf16x8 b, f32x4 c) { return __builtin_amdgcn_mfma_f32_16x16x32_bf16(a, b, c, 0, 0, 0); }
DI bf16x8 lds_b128(lptr p) { return *(LAS bf16x8*)p; }
DI f32x4 lds_f4(lptr p) { return *(LAS f32x4*)p; }
DI s16x4 lds_tr(lptr p) { return __builtin_amdgcn_ds_read_tr16_b64_v4i16((LAS s16x4*)p); }
DI bf16x8 lds_tr2(lptr p) { s16x4 a = lds_tr(p), b = lds_tr(p + 512); s16x8 v = __builtin_shufflevector(a, b, 0, 1, 2, 3, 4, 5, 6, 7); return __builtin_bit_cast(bf16x8, v); }
DI void glds16(const void* g, lptr l) { __builtin_amdgcn_global_load_lds((const unsigned*)g, (LAS unsigned*)l, 16, 0, 0); }
template <int N> DI void wait_vm() { asm volatile("s_waitcnt vmcnt(%0)" ::"n"(N) : "memory"); }
DI void wait_lgkm0() { __builtin_amdgcn_s_waitcnt(0xc07f); }
DI void sbar() { __builtin_amdgcn_sched_barrier(0); }
DI void bar() { asm volatile("" ::: "memory"); sbar(); __builtin_amdgcn_s_barrier(); sbar(); asm volatile("" ::: "memory"); }
DI float dot2(unsigned a, unsigned b, float c) { return __builtin_amdgcn_fdot2_f32_bf16(__builtin_bit_cast(bf16x2, a), __builtin_bit_cast(bf16x2, b), c, false); }
DI float bf_round(float f) { return (float)(__bf16)f; }
DI float bflo(unsigned u) { return __builtin_bit_cast(float, u << 16); }
DI float bfhi(unsigned u) { return __builtin_bit_cast(float, u & 0xffff0000u); }
DI bf16x8 pack8(f32x4 a, f32x4 b) { bf16x8 r; r[0] = (__bf16)a[0]; r[1] = (__bf16)a[1]; r[2] = (__bf16)a[2]; r[3] = (__bf16)a[3]; r[4] = (__bf16)b[0]; r[5] = (__bf16)b[1]; r[6] = (__bf16)b[2]; r[7] = (__bf16)b[3]; return r; }
DI bf16x4 pack4(f32x4 a) { bf16x4 r; r[0] = (__bf16)a[0]; r[1] = (__bf16)a[1]; r[2] = (__bf16)a[2]; r[3] = (__bf16)a[3]; return r; }
DI bf16x8 neg8(bf16x8 a) { u32x4 u = __builtin_bit_cast(u32x4, a); u ^= 0x80008000u; return __builtin_bit_cast(bf16x8, u); }
DI float sum_g(float v) {
  unsigned u = __builtin_bit_cast(unsigned, v);
  auto a = __builtin_amdgcn_permlane32_swap(u, u, false, false);
  v = __builtin_bit_cast(float, (unsigned)a[0]) + __builtin_bit_cast(float, (unsigned)a[1]);
  u = __builtin_bit_cast(unsigned, v);
  auto b = __builtin_amdgcn_permlane16_swap(u, u, false, false);
  return __builtin_bit_cast(float, (unsigned)b[0]) + __builtin_bit_cast(float, (unsigned)b[1]);
}
DI float bperm(float v, int src_lane) { return __builtin_bit_cast(float, __builtin_amdgcn_ds_bpermute(src_lane * 4, __builtin_bit_cast(int, v))); }
DI u32x4 ld16(rsrc_t r, int voff, int soff) { return __builtin_amdgcn_raw_buffer_load_b128(r, voff, soff, 16); }
DI void st16(u32x4 v, rsrc_t r, int voff, int soff) { __builtin_amdgcn_raw_buffer_store_b128(v, r, voff + soff, 0, 16); }
DI void st_flag(u32x2 ep, rsrc_t r, int off) { __builtin_amdgcn_raw_buffer_store_b64(ep, r, off, 0, 16); }

constexpr int S_ = 2048, D_ = 512, DFF = 1792;
constexpr int WS_E = 0, WS_F1 = 4096, WS_F2 = 8192, WS_F3 = 12288, WS_DBG = 16384;
constexpr int WS_AT = 131072;
constexpr int WS_WT = WS_AT + 256 * 8192;
constexpr int WS_WS = WS_WT + 256 * 8192;
constexpr int WS_H = WS_WS + 32768;
constexpr int WS_BT = WS_H + 128 * 56 * 1024;
constexpr int WS_END = WS_BT + 512 * 4096;
constexpr int UNIT = 8192, RING = 16 * UNIT, L_Y = RING, Y_P = 96, Y_IM = 64 * Y_P, L_V = L_Y + 2 * Y_IM, V_P = 144, V_IM = 32 * V_P, L_CS = L_V + 2 * V_IM, LDS_BYTES = L_CS + 512;
constexpr int U_W1 = 20, U_FC = 36, U_W2 = 43, U_G = 71, U_END = 79;
constexpr int SPIN = 4000;

#define TS(i) do { } while (0)

template <int N> DI void poll_flags(rsrc_t r, int voff, u32x2 ep) {
  for (int it = 0; it < SPIN; ++it) {
    bool ok;
    if constexpr (N == 0) { u32x2 v = __builtin_amdgcn_raw_buffer_load_b64(r, voff, 0, 16); ok = v[0] == ep[0] && v[1] == ep[1]; }
    else { ok = true;
#pragma unroll
      for (int k = 0; k < N; ++k) { u32x4 v = __builtin_amdgcn_raw_buffer_load_b128(r, voff + 16 * k, 0, 16); ok = ok && v[0] == ep[0] && v[1] == ep[1] && v[2] == ep[0] && v[3] == ep[1]; } }
    if (__builtin_amdgcn_ballot_w64(!ok) == 0) break;
    __builtin_amdgcn_s_sleep(2);
    asm volatile("" ::: "memory");
  }
  asm volatile("" ::: "memory");
}

DI void wait_units(int n) {
  if (n >= 8) { if (n >= 12) { if (n >= 14) wait_vm<56>(); else wait_vm<48>(); } else { if (n >= 10) wait_vm<40>(); else wait_vm<32>(); } }
  else { if (n >= 4) { if (n >= 6) wait_vm<24>(); else wait_vm<16>(); } else { if (n >= 2) wait_vm<8>(); else wait_vm<0>(); } }
}

DI void dma_role(lptr lds, const char* x, const char* w1, const char* w2, int id, int rb, int cg, int n2, int cb, int dw, int lane) {
  const int e = lane >> 3, cp = lane & 7;
  const unsigned vlin = lane * 16;
  const unsigned vx = (unsigned)(lane >> 4) * 32768u + (unsigned)(((lane & 15) ^ (lane >> 4)) * 16);
  const unsigned vch = (unsigned)((((cp >> 1) ^ ((e >> 1) & 1)) * 32) + (cp & 1) * 16);
  const unsigned vw1 = (unsigned)e * 3584u + vch;
  const unsigned vw2 = (unsigned)(16 * (e >> 2) + (e & 3)) * 1024u + vch;
  const int cgw = cg < 14 ? cg : 13;
  const char* xb = x + (size_t)(32 * (32 * (rb & 1) + 16 * dw) + 4 * (rb >> 1)) * 1024;
  const char* fb = (const char*)&d_FT + (size_t)cg * 32768 + dw * 4096;
  const char* w1b = w1 + (size_t)(32 * dw) * 3584 + cgw * 256;
  const char* cb_ = (const char*)&d_FC + dw * 4096;
  const char* w2b = w2 + (size_t)(32 * dw) * 1024 + cb * 128;
  const char* gb = (const char*)&d_GT + (size_t)cb * 65536 + dw * 4096;
  const int nspecial = id == 0 ? 3 : 0;
  const int NB = 34 + nspecial;
  int issued = 0, lim = 16, freed = 0;
  for (int b = 0;; ++b) {
    for (; issued < lim; ++issued) {
      const int u = issued;
      const char* sb; int st; unsigned v0, v1, v2, v3;
      if (u < U_W1) {
        const int s = u / 5, r = u - 5 * s;
        if (r == 0) { sb = fb + s * 8192; st = 1024; v0 = v1 = v2 = v3 = vlin; }
        else { sb = xb + (size_t)(r - 1) * 131072 + s * 256; st = 1024; v0 = vx; v1 = vx ^ 64u; v2 = vx ^ 128u; v3 = vx ^ 192u; }
      } else if (u < U_FC) {
        const int q = u - U_W1;
        sb = w1b + (size_t)(q & 7) * (64 * 3584) + (q >> 3) * 128; st = 8 * 3584; v0 = v2 = vw1; v1 = v3 = vw1 ^ 64u;
      } else if (u < U_W2) { sb = cb_ + (u - U_FC) * 8192; st = 1024; v0 = v1 = v2 = v3 = vlin; }
      else if (u < U_G) {
        const int q = u - U_W2, hb = q >= 14 ? 1 : 0, cgi = q - 14 * hb;
        sb = w2b + (size_t)(128 * cgi + 64 * hb) * 1024; st = 4096; v0 = v2 = vw2; v1 = v3 = vw2 ^ 64u;
      } else { sb = gb + (u - U_G) * 8192; st = 1024; v0 = v1 = v2 = v3 = vlin; }
      lptr dst = lds + (u & 15) * UNIT + dw * 4096;
      glds16(sb + v0, dst); glds16(sb + st + v1, dst + 1024); glds16(sb + 2 * st + v2, dst + 2048); glds16(sb + 3 * st + v3, dst + 3072);
    }
    if (b == NB) break;
    const int bb = b <= 13 ? b : (b <= 13 + nspecial ? 13 : b - nspecial);
    const int c = bb < 4 ? 5 : bb < 12 ? 2 : bb == 12 ? 7 : bb < 15 ? 0 : bb < 29 ? 2 : bb == 29 ? 0 : 2;
    wait_units(issued - (freed + c));
    bar();
    lim = freed + 16 < U_END ? freed + 16 : U_END;
    freed += c;
  }
}

DI void compute_role(lptr lds, const uint16_t* __restrict__ x, const uint16_t* __restrict__ wr, const uint16_t* __restrict__ wi, char* ws, uint16_t* __restrict__ out,
                     int id, int rb, int cg, int n2t, int cb, int w, int lane) {
  const int g = lane >> 4, li = lane & 15;
  [[maybe_unused]] const int wid_ts = w;
  const rsrc_t rs = __builtin_amdgcn_make_buffer_rsrc((void*)ws, 0, WS_END, 0x00020000);
  TS(0);
  u32x2 ep;
  { u32x2 e0 = __builtin_amdgcn_raw_buffer_load_b64(rs, WS_E, 0, 16);
    u64 E = ((u64)(unsigned)__builtin_amdgcn_readfirstlane(e0[1]) << 32) | (unsigned)__builtin_amdgcn_readfirstlane(e0[0]);
    E = E * 6364136223846793005ull + 1442695040888963407ull;
    ep[0] = (unsigned)E; ep[1] = (unsigned)(E >> 32); }
  const int wk = rb >> 2, gk = rb & 3;
  unsigned wv[8];
  { const int k1 = 16 * wk + 4 * w + g, j = 16 * cg + li;
    uint16_t a[8], b[8];
#pragma unroll
    for (int e = 0; e < 8; ++e) { const int k = k1 + 64 * (16 * (e >> 2) + 4 * gk + (e & 3)); a[e] = wr[k * 257 + j]; b[e] = wi[k * 257 + j]; }
#pragma unroll
    for (int e = 0; e < 8; ++e) wv[e] = (unsigned)a[e] | ((unsigned)b[e] << 16);
    if (cg == 0 && li < 8) {
      const int k = k1 + 64 * (16 * (li >> 2) + 4 * gk + (li & 3)), km = (2048 - k) & 2047;
      const float w0r = bflo(wr[k * 257]), w0i = bflo(wi[k * 257]), m0r = bflo(wr[km * 257]), m0i = bflo(wi[km * 257]);
      const float wnr = bflo(wr[k * 257 + 256]), wni = bflo(wi[k * 257 + 256]), mnr = bflo(wr[km * 257 + 256]), mni = bflo(wi[km * 257 + 256]);
      const float ar = 0.5f * (w0r + m0r), ai = 0.5f * (w0i - m0i), br = 0.5f * (wnr + mnr), bi = 0.5f * (wni - mni);
      const f32x4 uv = {0.25f * (ar + br), 0.25f * (ai + bi), 0.25f * (ar - br), 0.25f * (ai - bi)};
      st16(__builtin_bit_cast(u32x4, uv), rs, WS_WS + ((wk * 8 + li) * 64 + 16 * gk + 4 * w + g) * 16, 0);
    } }
  bf16x8 xr[2][16];
  f32x4 acc[2][4];
  const f32x4 z4 = {0.f, 0.f, 0.f, 0.f};
  float sum[2] = {0.f, 0.f}, sq[2] = {0.f, 0.f};
#pragma unroll
  for (int m = 0; m < 2; ++m) { acc[m][0] = z4; acc[m][1] = z4; }
  const int xa = li * 256 + ((g ^ li) * 16);
  sfor<4>([&](auto ST) {
    constexpr int st = ST;
    bar();
    if constexpr (st == 0) TS(1);
    lptr fs = lds + ((5 * st) & 15) * UNIT + lane * 16;
    const int xs = ((5 * st + 1 + w) & 15) * UNIT;
    bf16x8 ff[2][4];
#pragma unroll
    for (int k = 0; k < 4; ++k) {
      xr[0][4 * st + k] = lds_b128(lds + xs + (xa ^ (k << 6))); ff[0][k] = lds_b128(fs + k * 1024); ff[1][k] = lds_b128(fs + 4096 + k * 1024);
      xr[1][4 * st + k] = lds_b128(lds + xs + (xa ^ (k << 6)) + 4096);
    }
#pragma unroll
    for (int k = 0; k < 4; ++k)
#pragma unroll
      for (int m = 0; m < 2; ++m) {
        acc[m][0] = mfma(xr[m][4 * st + k], ff[0][k], acc[m][0]); acc[m][1] = mfma(xr[m][4 * st + k], ff[1][k], acc[m][1]);
        const u32x4 a = __builtin_bit_cast(u32x4, xr[m][4 * st + k]);
#pragma unroll
        for (int e = 0; e < 4; ++e) { sum[m] = dot2(a[e], 0x3f803f80u, sum[m]); sq[m] = dot2(a[e], a[e], sq[m]); }
      }
    wait_lgkm0();
  });
  const int ta = (8 * g + (li >> 2)) * 128 + (li & 3) * 8 + ((((li >> 3) & 1) + 2 * (g & 1)) * 32);
  bf16x8 wf[2][4];
  int sb = (U_W1 & 15) * UNIT;
  auto rd2 = [&](int buf, int base, int ksl) {
#pragma unroll
    for (int t = 0; t < 4; ++t) wf[buf][t] = lds_tr2(lds + base + ((ta ^ (w << 5)) ^ (t << 5)) + ksl * 4096);
  };
  bar();
  TS(2);
  rd2(0, sb, 0);
  sbar();
  float mu[2], rsd[2];
  {
#pragma unroll
    for (int m = 0; m < 2; ++m) {
      const float s1 = sum_g(sum[m]), s2 = sum_g(sq[m]);
      const float mean = s1 * (1.f / 512.f);
      const float var = fmaxf(s2 * (1.f / 512.f) - mean * mean, 0.f);
      const float den = bf_round(sqrtf(bf_round(bf_round(var) + 1e-5f)));
      mu[m] = bf_round(mean); rsd[m] = 1.f / den;
    }
    f32x4 v[2][2];
#pragma unroll
    for (int m = 0; m < 2; ++m) {
      f32x4 r4, m4;
#pragma unroll
      for (int r = 0; r < 4; ++r) { r4[r] = bperm(rsd[m], 4 * g + r); m4[r] = bperm(mu[m], 4 * g + r); }
      const float csf = (cg == 0 && li == 0) ? 512.f : 0.f;
      v[m][0] = (acc[m][0] - m4 * csf) * r4; v[m][1] = acc[m][1] * r4;
    }
    const int ao = WS_AT + (16 * cg + li) * 8192 + (rb >> 1) * 512 + (rb & 1) * 256 + w * 64 + g * 16;
    st16(__builtin_bit_cast(u32x4, pack8(v[0][0], v[1][0])), rs, ao, 0);
    st16(__builtin_bit_cast(u32x4, pack8(v[0][1], v[1][1])), rs, ao + 4096, 0);
    const int wo = WS_WT + (16 * cg + li) * 8192 + wk * 2048 + (16 * gk + 4 * w + g) * 16;
    st16(u32x4{wv[0], wv[1], wv[2], wv[3]}, rs, wo, 0);
    st16(u32x4{wv[4], wv[5], wv[6], wv[7]}, rs, wo + 1024, 0);
  }
  TS(3);
  for (int hb = 0; hb < 2; ++hb) {
#pragma unroll
    for (int m = 0; m < 2; ++m)
#pragma unroll
      for (int t = 0; t < 4; ++t) acc[m][t] = z4;
    float cs = 0.f;
    sfor<16>([&](auto KS) {
      constexpr int ks = KS, cur = ks & 1, nxt = cur ^ 1;
      sbar();
      if constexpr (ks < 15) {
        if constexpr ((ks & 3) == 3) {
          wait_lgkm0();
          if constexpr (ks == 7) wait_vm<0>();
          bar();
          if constexpr (ks == 7) { if (w == 0 && lane == 0) st_flag(ep, rs, hb == 0 ? WS_F1 + (cg * 16 + rb) * 8 : WS_F2 + (rb * 16 + cg) * 8); }
        }
        if constexpr (((ks + 1) & 1) == 0) sb = (sb + UNIT) & (RING - 1);
        rd2(nxt, sb, (ks + 1) & 1);
      }
#pragma unroll
      for (int t = 0; t < 4; ++t)
#pragma unroll
        for (int m = 0; m < 2; ++m) acc[m][t] = mfma(wf[cur][t], xr[m][ks], acc[m][t]);
      { const u32x4 a = __builtin_bit_cast(u32x4, wf[cur][0]);
#pragma unroll
        for (int e = 0; e < 4; ++e) cs = dot2(a[e], 0x3f803f80u, cs); }
      if constexpr (ks < 15) {
#pragma unroll
        for (int i = 0; i < 8; ++i) { __builtin_amdgcn_sched_group_barrier(0x008, 1, 0); __builtin_amdgcn_sched_group_barrier(0x100, 1, 0); }
      }
    });
    sbar();
    cs = sum_g(cs);
    if (lane < 16) *(LAS float*)(lds + L_CS + hb * 256 + w * 64 + lane * 4) = cs;
    wait_lgkm0();
    bar();
    sb = (sb + UNIT) & (RING - 1);
    if (hb == 0) rd2(0, sb, 0);
    sbar();
    if (cg < 14) {
      const bool odd = w & 1;
#pragma unroll
      for (int m = 0; m < 2; ++m)
#pragma unroll
        for (int u = 0; u < 2; ++u) {
          f32x4 v[2];
#pragma unroll
          for (int t = 0; t < 2; ++t) {
            const f32x4 c4 = lds_f4(lds + L_CS + hb * 256 + (((2 * u + t) ^ w) * 64) + g * 16);
            v[t] = (acc[m][2 * u + t] - c4 * mu[m]) * rsd[m];
#pragma unroll
            for (int e = 0; e < 4; ++e) v[t][e] = fmaxf(v[t][e], 0.f);
          }
          const u32x2 p0 = __builtin_bit_cast(u32x2, pack4(v[0])), p1 = __builtin_bit_cast(u32x2, pack4(v[1]));
          const u32x4 o = {odd ? p1[0] : p0[0], odd ? p1[1] : p0[1], odd ? p0[0] : p1[0], odd ? p0[1] : p1[1]};
          const int ks56 = hb * 28 + 2 * cg + (u ^ (w >> 1));
          st16(o, rs, WS_H + (((4 * (rb >> 1) + (li >> 2)) * 4 + 2 * (rb & 1) + m) * 56) * 1024 + (16 * g + 4 * w + (li & 3)) * 16, ks56 * 1024);
        }
    }
    TS(4 + hb);
  }
  {
    const int j = id, u = w & 1, v = w >> 1;
    lptr fc = lds + (U_FC & 15) * UNIT + lane * 16;
    bf16x8 c64[2][2][2], c32[2][2];
    f32x4 tw1[2][2], tw3[2][2];
#pragma unroll
    for (int t = 0; t < 2; ++t)
#pragma unroll
      for (int p = 0; p < 2; ++p) {
#pragma unroll
        for (int ks = 0; ks < 2; ++ks) c64[t][ks][p] = lds_b128(fc + v * 8192 + ((t * 2 + ks) * 2 + p) * 1024);
        c32[t][p] = lds_b128(fc + 49152 + (t * 2 + p) * 1024);
        tw1[t][p] = lds_f4(fc + 16384 + w * 4096 + (t * 2 + p) * 1024);
        tw3[t][p] = lds_f4(fc + 32768 + w * 4096 + (t * 2 + p) * 1024);
      }
    poll_flags<0>(rs, WS_F1 + ((j >> 4) * 16 + li) * 8, ep);
    TS(6);
    bf16x8 zr[2], zi[2];
    const int zo = WS_AT + j * 8192 + (4 * u + (li >> 2)) * 512 + g * 64 + (li & 3) * 16;
#pragma unroll
    for (int ks = 0; ks < 2; ++ks) { zr[ks] = __builtin_bit_cast(bf16x8, ld16(rs, zo + ks * 256, 0)); zi[ks] = __builtin_bit_cast(bf16x8, ld16(rs, zo + 4096 + ks * 256, 0)); }
    u32x4 wq[2];
    wq[0] = ld16(rs, WS_WT + j * 8192 + w * 2048 + lane * 16, 0); wq[1] = ld16(rs, WS_WT + j * 8192 + w * 2048 + 1024 + lane * 16, 0);
#pragma unroll
    for (int t = 0; t < 2; ++t) {
      f32x4 yr = z4, yi = z4;
#pragma unroll
      for (int ks = 0; ks < 2; ++ks) {
        yr = mfma(zr[ks], c64[t][ks][0], yr); yr = mfma(neg8(zi[ks]), c64[t][ks][1], yr);
        yi = mfma(zi[ks], c64[t][ks][0], yi); yi = mfma(zr[ks], c64[t][ks][1], yi);
      }
      const f32x4 pr = yr * tw1[t][0] + yi * tw1[t][1], pi = yi * tw1[t][0] - yr * tw1[t][1];
      lptr p = lds + L_Y + (32 * v + 8 * (li >> 2) + 4 * t + (li & 3)) * Y_P + (8 * g + 4 * u) * 2;
      *(LAS bf16x4*)p = pack4(pr); *(LAS bf16x4*)(p + Y_IM) = pack4(pi);
    }
    wait_vm<0>();
    wait_lgkm0();
    bar();
    if (w == 0 && lane == 0) st_flag(ep, rs, WS_F2 + 2048 + (rb * 16 + cg) * 8);
    f32x4 xre[2], xim[2];
    {
      lptr p = lds + L_Y + (16 * w + li) * Y_P + g * 16;
      const bf16x8 ur = lds_b128(p), ui = lds_b128(p + Y_IM), nui = neg8(ui);
#pragma unroll
      for (int t = 0; t < 2; ++t) {
        xre[t] = mfma(c32[t][0], ur, z4); xre[t] = mfma(c32[t][1], nui, xre[t]);
        xim[t] = mfma(c32[t][1], ur, z4); xim[t] = mfma(c32[t][0], ui, xim[t]);
      }
    }
    f32x4 pr[2], pi[2];
    if (j != 0) {
#pragma unroll
      for (int t = 0; t < 2; ++t)
#pragma unroll
        for (int r = 0; r < 4; ++r) {
          const float wre = bflo(wq[t][r]), wim = bfhi(wq[t][r]);
          pr[t][r] = xre[t][r] * wre - xim[t][r] * wim; pi[t][r] = xre[t][r] * wim + xim[t][r] * wre;
        }
    } else {
      f32x4 uv[2][4];
#pragma unroll
      for (int t = 0; t < 2; ++t)
#pragma unroll
        for (int r = 0; r < 4; ++r) uv[t][r] = __builtin_bit_cast(f32x4, ld16(rs, WS_WS + ((w * 8 + 4 * t + r) * 64 + lane) * 16, 0));
      wait_lgkm0();
      bar();
      LAS f32x2* X = (LAS f32x2*)(lds + L_Y);
#pragma unroll
      for (int t = 0; t < 2; ++t)
#pragma unroll
        for (int r = 0; r < 4; ++r) X[16 * w + li + 64 * (16 * t + 4 * g + r)] = f32x2{xre[t][r], xim[t][r]};
      wait_lgkm0();
      bar();
#pragma unroll
      for (int t = 0; t < 2; ++t)
#pragma unroll
        for (int r = 0; r < 4; ++r) {
          const int k = 16 * w + li + 64 * (16 * t + 4 * g + r);
          const f32x2 xm = X[(2048 - k) & 2047];
          const f32x4 c = uv[t][r];
          pr[t][r] = xre[t][r] * c[0] - xim[t][r] * c[1] + xm[0] * c[2] + xm[1] * c[3];
          pi[t][r] = xre[t][r] * c[1] + xim[t][r] * c[0] + xm[0] * c[3] - xm[1] * c[2];
        }
      wait_lgkm0();
      bar();
    }
    {
      const bf16x8 Pr = pack8(pr[0], pr[1]), Pi = pack8(pi[0], pi[1]), nPr = neg8(Pr);
#pragma unroll
      for (int t = 0; t < 2; ++t) {
        f32x4 vr = mfma(Pr, c32[t][0], z4); vr = mfma(Pi, c32[t][1], vr);
        f32x4 vi = mfma(Pi, c32[t][0], z4); vi = mfma(nPr, c32[t][1], vi);
        const f32x4 ar = vr * tw3[t][0] - vi * tw3[t][1], ai = vr * tw3[t][1] + vi * tw3[t][0];
        lptr p = lds + L_V + (16 * t + li) * V_P + (32 * (w >> 1) + 8 * g + 4 * (w & 1)) * 2;
        *(LAS bf16x4*)p = pack4(ar); *(LAS bf16x4*)(p + V_IM) = pack4(ai);
      }
    }
    wait_lgkm0();
    bar();
    {
      bf16x8 vr[2], vi[2];
#pragma unroll
      for (int ks = 0; ks < 2; ++ks) { lptr p = lds + L_V + (16 * u + li) * V_P + (32 * ks + 8 * g) * 2; vr[ks] = lds_b128(p); vi[ks] = lds_b128(p + V_IM); }
      f32x4 yr[2], yi[2];
#pragma unroll
      for (int t = 0; t < 2; ++t) {
        yr[t] = z4; yi[t] = z4;
#pragma unroll
        for (int ks = 0; ks < 2; ++ks) {
          yr[t] = mfma(c64[t][ks][0], vr[ks], yr[t]); yr[t] = mfma(c64[t][ks][1], vi[ks], yr[t]);
          yi[t] = mfma(c64[t][ks][0], vi[ks], yi[t]); yi[t] = mfma(c64[t][ks][1], neg8(vr[ks]), yi[t]);
        }
      }
      const int cre = 32 * (j >> 4) + (j & 15);
      const int bo = WS_BT + cre * 4096 + (16 * u + li) * 128 + (32 * v + 8 * g) * 2;
      st16(__builtin_bit_cast(u32x4, pack8(yr[0], yr[1])), rs, bo, 0);
      st16(__builtin_bit_cast(u32x4, pack8(yi[0], yi[1])), rs, bo + 16 * 4096, 0);
    }
    TS(7);
  }
  f32x4 acc1[4], acc2[4];
  {
    wait_vm<0>();
    wait_lgkm0();
    bar();
    if (w == 0 && lane == 0) st_flag(ep, rs, WS_F3 + id * 8);
    poll_flags<0>(rs, WS_F2 + ((2 * (n2t >> 2)) * 16 + (lane & 31)) * 8, ep);
    poll_flags<0>(rs, WS_F2 + 2048 + ((2 * (n2t >> 2)) * 16 + (lane & 31)) * 8, ep);
    TS(8);
    const int ho = WS_H + (n2t * 4 + w) * 56 * 1024 + lane * 16;
    bf16x8 hr[28];
#pragma unroll
    for (int i = 0; i < 28; ++i) hr[i] = __builtin_bit_cast(bf16x8, ld16(rs, ho, i * 1024));
    bf16x8 wg[2][8];
    sb = (U_W2 & 15) * UNIT;
    auto rd4 = [&](int buf, int base) {
#pragma unroll
      for (int k = 0; k < 2; ++k)
#pragma unroll
        for (int t = 0; t < 4; ++t) wg[buf][k * 4 + t] = lds_tr2(lds + base + (ta ^ (t << 5)) + k * 4096);
    };
    rd4(0, sb);
#pragma unroll
    for (int t = 0; t < 4; ++t) acc1[t] = z4;
    for (int it = 0; it < 2; ++it) {
      sfor<14>([&](auto UN) {
        constexpr int un = UN, cur = un & 1, nxt = cur ^ 1;
        sbar();
        if constexpr (un & 1) { wait_lgkm0(); bar(); }
        sb = (sb + UNIT) & (RING - 1);
        if (!(un == 13 && it == 1)) rd4(nxt, sb);
#pragma unroll
        for (int k = 0; k < 2; ++k)
#pragma unroll
          for (int t = 0; t < 4; ++t) acc1[t] = mfma(wg[cur][k * 4 + t], hr[2 * un + k], acc1[t]);
        if (it == 0) { hr[2 * un] = __builtin_bit_cast(bf16x8, ld16(rs, ho, (28 + 2 * un) * 1024)); hr[2 * un + 1] = __builtin_bit_cast(bf16x8, ld16(rs, ho, (29 + 2 * un) * 1024)); }
#pragma unroll
        for (int i = 0; i < 8; ++i) { __builtin_amdgcn_sched_group_barrier(0x008, 1, 0); __builtin_amdgcn_sched_group_barrier(0x100, 2, 0); }
      });
    }
    sbar();
    TS(9);
  }
  const int nrow = 32 * (16 * w + li) + n2t;
  u32x2 xv[4];
#pragma unroll
  for (int t = 0; t < 4; ++t) xv[t] = *(const u32x2*)(x + (size_t)nrow * 512 + 64 * cb + 16 * t + 4 * g);
  {
    poll_flags<2>(rs, WS_F3 + lane * 32, ep);
    TS(10);
    if (id == 0 && w == 0 && lane == 0) st_flag(ep, rs, WS_E);
    u32x4 bq[16];
    const int e8 = lane >> 3, cp = lane & 7;
    const int bo = WS_BT + (16 * w + e8) * 4096 + n2t * 128 + cp * 16;
#pragma unroll
    for (int b = 0; b < 8; ++b) { bq[2 * b] = ld16(rs, bo, b * 64 * 4096); bq[2 * b + 1] = ld16(rs, bo, b * 64 * 4096 + 8 * 4096); }
    const int lo = (16 * w + e8) * 128 + (((cp >> 1) ^ ((e8 >> 1) & 1)) * 32) + (cp & 1) * 16;
#pragma unroll
    for (int b = 0; b < 8; ++b) {
      lptr p = lds + ((U_END + b) & 15) * UNIT;
      *(LAS u32x4*)(p + lo) = bq[2 * b]; *(LAS u32x4*)(p + ((lo + 1024) ^ 64)) = bq[2 * b + 1];
    }
    wait_lgkm0();
    bar();
    TS(11);
#pragma unroll
    for (int t = 0; t < 4; ++t) acc2[t] = z4;
    for (int sp = 0; sp < 4; ++sp) {
      if (sp > 0) { wait_lgkm0(); bar(); }
#pragma unroll
      for (int h = 0; h < 2; ++h) {
        const int b = 2 * sp + h;
        lptr gs = lds + ((U_G + b) & 15) * UNIT + lane * 16;
        lptr bs = lds + ((U_END + b) & 15) * UNIT + (ta ^ (w << 5));
        bf16x8 gf[8], bf[2];
#pragma unroll
        for (int k = 0; k < 2; ++k) {
          bf[k] = lds_tr2(bs + k * 4096);
#pragma unroll
          for (int t = 0; t < 4; ++t) gf[k * 4 + t] = lds_b128(gs + (t * 2 + k) * 1024);
        }
#pragma unroll
        for (int k = 0; k < 2; ++k)
#pragma unroll
          for (int t = 0; t < 4; ++t) acc2[t] = mfma(gf[k * 4 + t], bf[k], acc2[t]);
      }
    }
  }
  TS(12);
  u32x2 o[4];
#pragma unroll
  for (int t = 0; t < 4; ++t) {
    f32x4 r4;
#pragma unroll
    for (int r = 0; r < 4; ++r) {
      const float v = bf_round(acc1[t][r]);
      const float gate = bf_round(v / (1.f + __expf(-v)));
      const float sv = bf_round(acc2[t][r] * (1.f / 524288.f));
      const float xin = (r & 1) ? bfhi(xv[t][r >> 1]) : bflo(xv[t][r >> 1]);
      r4[r] = xin + bf_round(sv * gate);
    }
    o[t] = __builtin_bit_cast(u32x2, pack4(r4));
  }
#pragma unroll
  for (int u = 0; u < 2; ++u) {
    auto s0 = __builtin_amdgcn_permlane16_swap(o[2 * u][0], o[2 * u + 1][0], false, false);
    auto s1 = __builtin_amdgcn_permlane16_swap(o[2 * u][1], o[2 * u + 1][1], false, false);
    const u32x4 q = {(unsigned)s0[0], (unsigned)s1[0], (unsigned)s0[1], (unsigned)s1[1]};
    *(u32x4*)(out + (size_t)nrow * 512 + 64 * cb + 32 * u + 16 * (g & 1) + 8 * (g >> 1)) = q;
  }
  TS(13);
}

__global__ __launch_bounds__(384, 2) void fused_k(const uint16_t* __restrict__ x, const uint16_t* __restrict__ wr, const uint16_t* __restrict__ wi,
                                                   const uint16_t* __restrict__ w1, const uint16_t* __restrict__ w2, char* __restrict__ ws, uint16_t* __restrict__ out) {
  extern __shared__ __attribute__((aligned(16))) char smem[];
  lptr lds = (lptr)smem;
  const int wid = __builtin_amdgcn_readfirstlane(threadIdx.x >> 6), lane = threadIdx.x & 63;
  const int id = blockIdx.x, xcd = id & 7, loc = id >> 3;
  const int rb = 8 * (xcd & 1) + (loc & 7), cg = 4 * (xcd >> 1) + (loc >> 3);
  const int n2t = 4 * xcd + (loc & 3), cb = loc >> 2;
  if (wid >= 4) dma_role(lds, (const char*)x, (const char*)w1, (const char*)w2, id, rb, cg, n2t, cb, wid - 4, lane);
  else compute_role(lds, x, wr, wi, ws, out, id, rb, cg, n2t, cb, wid, lane);
}

extern "C" void kernel_launch(void* const* d_in, const int* in_sizes, int n_in, void* d_out, int out_size, void* d_ws, size_t ws_size, hipStream_t stream) {
  hipFuncSetAttribute((const void*)fused_k, hipFuncAttributeMaxDynamicSharedMemorySize, LDS_BYTES);
  fused_k<<<256, 384, LDS_BYTES, stream>>>((const uint16_t*)d_in[0], (const uint16_t*)d_in[1], (const uint16_t*)d_in[2], (const uint16_t*)d_in[4], (const uint16_t*)d_in[5],
                                           (char*)d_ws, (uint16_t*)d_out);
}
```

```cpp
#include <hip/hip_runtime.h>
#include <stdint.h>
#include <utility>

#define DI __device__ __forceinline__
typedef __attribute__((ext_vector_type(8))) __bf16 bf16x8;
typedef __attribute__((ext_vector_type(4))) __bf16 bf16x4;
typedef __attribute__((ext_vector_type(2))) __bf16 bf16x2;
typedef __attribute__((ext_vector_type(4))) float f32x4;
typedef __attribute__((ext_vector_type(2))) float f32x2;
typedef __attribute__((ext_vector_type(4))) short s16x4;
typedef __attribute__((ext_vector_type(8))) short s16x8;
typedef __attribute__((ext_vector_type(4))) unsigned u32x4;
typedef __attribute__((ext_vector_type(2))) unsigned u32x2;
typedef unsigned long long u64;
#define LAS __attribute__((address_space(3)))
typedef LAS char* lptr;
typedef __amdgpu_buffer_rsrc_t rsrc_t;

template <class F, int... I> DI void sfor_impl(F&& f, std::integer_sequence<int, I...>) { (f(std::integral_constant<int, I>{}), ...); }
template <int N, class F> DI void sfor(F&& f) { sfor_impl(f, std::make_integer_sequence<int, N>{}); }

constexpr double kPi = 3.14159265358979323846264338327950288;
constexpr double c_cos(double x) { double x2 = x * x, term = 1.0, sum = 1.0; for (int i = 1; i < 18; ++i) { term *= -x2 / ((2 * i - 1) * (2 * i)); sum += term; } return sum; }
constexpr uint16_t c_f2bf(float f) { uint32_t u = __builtin_bit_cast(uint32_t, f); u += 0x7fffu + ((u >> 16) & 1u); return (uint16_t)(u >> 16); }
struct Cos2048 { float v[2048]; constexpr Cos2048() : v{} { for (int m = 0; m < 2048; ++m) { int mm = m <= 1024 ? m : 2048 - m; double c = c_cos(2.0 * kPi * mm / 2048.0); if (mm == 512) c = 0.0; v[m] = (float)c; } } };
constexpr Cos2048 C2K{};
struct Cos512 { uint16_t v[512]; constexpr Cos512() : v{} { for (int m = 0; m < 512; ++m) v[m] = c_f2bf(C2K.v[4 * m]); } };
constexpr Cos512 C512{};
constexpr uint16_t fval(int d, int c) { const int T = c >> 4, j = 16 * (T >> 1) + (c & 15); const int m = (T & 1) == 0 ? ((d * j) & 511) : (j == 0 ? (d & 1) * 256 : ((d * j + 128) & 511)); return C512.v[m]; }
constexpr int c_sl(int g, int e) { return 16 * (e >> 2) + 4 * g + (e & 3); }
constexpr int c_rho(int v, int t, int li) { return 32 * v + 8 * (li >> 2) + 4 * t + (li & 3); }

template <int P> struct FPart { uint16_t v[16384];
  constexpr FPart() : v{} { for (int cq = 0; cq < 4; ++cq) for (int t = 0; t < 2; ++t) for (int k = 0; k < 4; ++k) for (int l = 0; l < 64; ++l) for (int e = 0; e < 8; ++e)
    v[(((cq * 2 + t) * 4 + k) * 64 + l) * 8 + e] = fval(32 * k + 8 * (l >> 4) + e, 32 * (4 * P + cq) + 16 * t + (l & 15)); } };
template <int P> struct GPart { uint16_t v[16384];
  constexpr GPart() : v{} { for (int s = 0; s < 4; ++s) for (int t = 0; t < 4; ++t) for (int k = 0; k < 2; ++k) for (int l = 0; l < 64; ++l) for (int e = 0; e < 8; ++e)
    v[(((s * 4 + t) * 2 + k) * 64 + l) * 8 + e] = fval(64 * (P >> 1) + 16 * t + (l & 15), 64 * (4 * (P & 1) + s) + 32 * k + 8 * (l >> 4) + e); } };
struct FFTC { uint16_t c64[8192]; float tw1[4096]; uint16_t c32[4096]; float tw3[4096];
  constexpr FFTC() : c64{}, tw1{}, c32{}, tw3{} {
    for (int l = 0; l < 64; ++l) { const int g = l >> 4, li = l & 15;
      for (int p = 0; p < 2; ++p) {
        for (int e = 0; e < 8; ++e) {
          for (int v = 0; v < 2; ++v) for (int t = 0; t < 2; ++t) for (int ks = 0; ks < 2; ++ks)
            c64[((((v * 2 + t) * 2 + ks) * 2 + p) * 64 + l) * 8 + e] = C512.v[(8 * c_rho(v, t, li) * (32 * ks + c_sl(g, e)) + 128 * p) & 511];
          for (int t = 0; t < 2; ++t) c32[((t * 2 + p) * 64 + l) * 8 + e] = C512.v[(16 * (16 * t + li) * c_sl(g, e) + 128 * p) & 511];
        }
        for (int w = 0; w < 4; ++w) for (int t = 0; t < 2; ++t) for (int r = 0; r < 4; ++r) {
          tw1[(((w * 2 + t) * 2 + p) * 64 + l) * 4 + r] = C2K.v[((16 * (w & 1) + 4 * g + r) * c_rho(w >> 1, t, li) + 1536 * p) & 2047];
          tw3[(((w * 2 + t) * 2 + p) * 64 + l) * 4 + r] = C2K.v[((16 * w + 4 * g + r) * (16 * t + li) + 1536 * p) & 2047];
        } } } } };
struct FBig { FPart<0> a0; FPart<1> a1; FPart<2> a2; FPart<3> a3; };
struct GBig { GPart<0> a0; GPart<1> a1; GPart<2> a2; GPart<3> a3; GPart<4> a4; GPart<5> a5; GPart<6> a6; GPart<7> a7; GPart<8> a8; GPart<9> a9; GPart<10> a10; GPart<11> a11; GPart<12> a12; GPart<13> a13; GPart<14> a14; GPart<15> a15; };
#ifdef __HIP_DEVICE_COMPILE__
#define FP(n) constexpr FPart<n> fp##n{};
FP(0) FP(1) FP(2) FP(3)
#define GP(n) constexpr GPart<n> gp##n{};
GP(0) GP(1) GP(2) GP(3) GP(4) GP(5) GP(6) GP(7) GP(8) GP(9) GP(10) GP(11) GP(12) GP(13) GP(14) GP(15)
__device__ const FBig d_FT = {fp0, fp1, fp2, fp3};
__device__ const GBig d_GT = {gp0, gp1, gp2, gp3, gp4, gp5, gp6, gp7, gp8, gp9, gp10, gp11, gp12, gp13, gp14, gp15};
__device__ const FFTC d_FC = FFTC();
#else
__device__ const uint16_t d_FT[4 * 16384] = {0};
__device__ const uint16_t d_GT[16 * 16384] = {0};
__device__ const uint16_t d_FC[28672] = {0};
#endif

DI f32x4 mfma(bf16x8 a, bf16x8 b, f32x4 c) { return __builtin_amdgcn_mfma_f32_16x16x32_bf16(a, b, c, 0, 0, 0); }
DI bf16x8 lds_b128(lptr p) { return *(LAS bf16x8*)p; }
DI f32x4 lds_f4(lptr p) { return *(LAS f32x4*)p; }
DI s16x4 lds_tr(lptr p) { return __builtin_amdgcn_ds_read_tr16_b64_v4i16((LAS s16x4*)p); }
DI bf16x8 lds_tr2(lptr p) { s16x4 a = lds_tr(p), b = lds_tr(p + 512); s16x8 v = __builtin_shufflevector(a, b, 0, 1, 2, 3, 4, 5, 6, 7); return __builtin_bit_cast(bf16x8, v); }
DI void glds16(const void* g, lptr l) { __builtin_amdgcn_global_load_lds((const unsigned*)g, (LAS unsigned*)l, 16, 0, 0); }
template <int N> DI void wait_vm() { asm volatile("s_waitcnt vmcnt(%0)" ::"n"(N) : "memory"); }
DI void wait_lgkm0() { __builtin_amdgcn_s_waitcnt(0xc07f); }
DI void sbar() { __builtin_amdgcn_sched_barrier(0); }
DI void bar() { asm volatile("" ::: "memory"); sbar(); __builtin_amdgcn_s_barrier(); sbar(); asm volatile("" ::: "memory"); }
DI float dot2(unsigned a, unsigned b, float c) { return __builtin_amdgcn_fdot2_f32_bf16(__builtin_bit_cast(bf16x2, a), __builtin_bit_cast(bf16x2, b), c, false); }
DI float bf_round(float f) { return (float)(__bf16)f; }
DI float bflo(unsigned u) { return __builtin_bit_cast(float, u << 16); }
DI float bfhi(unsigned u) { return __builtin_bit_cast(float, u & 0xffff0000u); }
DI bf16x8 pack8(f32x4 a, f32x4 b) { bf16x8 r; r[0] = (__bf16)a[0]; r[1] = (__bf16)a[1]; r[2] = (__bf16)a[2]; r[3] = (__bf16)a[3]; r[4] = (__bf16)b[0]; r[5] = (__bf16)b[1]; r[6] = (__bf16)b[2]; r[7] = (__bf16)b[3]; return r; }
DI bf16x4 pack4(f32x4 a) { bf16x4 r; r[0] = (__bf16)a[0]; r[1] = (__bf16)a[1]; r[2] = (__bf16)a[2]; r[3] = (__bf16)a[3]; return r; }
DI bf16x8 neg8(bf16x8 a) { u32x4 u = __builtin_bit_cast(u32x4, a); u ^= 0x80008000u; return __builtin_bit_cast(bf16x8, u); }
DI float sum_g(float v) {
  unsigned u = __builtin_bit_cast(unsigned, v);
  auto a = __builtin_amdgcn_permlane32_swap(u, u, false, false);
  v = __builtin_bit_cast(float, (unsigned)a[0]) + __builtin_bit_cast(float, (unsigned)a[1]);
  u = __builtin_bit_cast(unsigned, v);
  auto b = __builtin_amdgcn_permlane16_swap(u, u, false, false);
  return __builtin_bit_cast(float, (unsigned)b[0]) + __builtin_bit_cast(float, (unsigned)b[1]);
}
DI float bperm(float v, int src_lane) { return __builtin_bit_cast(float, __builtin_amdgcn_ds_bpermute(src_lane * 4, __builtin_bit_cast(int, v))); }
DI u32x4 ld16(rsrc_t r, int voff, int soff) { return __builtin_amdgcn_raw_buffer_load_b128(r, voff, soff, 16); }
DI void st16(u32x4 v, rsrc_t r, int voff, int soff) { __builtin_amdgcn_raw_buffer_store_b128(v, r, voff + soff, 0, 16); }
DI void st_flag(u32x2 ep, rsrc_t r, int off) { __builtin_amdgcn_raw_buffer_store_b64(ep, r, off, 0, 16); }

constexpr int S_ = 2048, D_ = 512, DFF = 1792;
constexpr int WS_E = 0, WS_F1 = 4096, WS_F2 = 8192, WS_F3 = 12288, WS_DBG = 16384;
constexpr int WS_AT = 131072;
constexpr int WS_WT = WS_AT + 256 * 8192;
constexpr int WS_WS = WS_WT + 256 * 8192;
constexpr int WS_H = WS_WS + 32768;
constexpr int WS_BT = WS_H + 128 * 56 * 1024;
constexpr int WS_END = WS_BT + 512 * 4096;
constexpr int UNIT = 8192, RING = 16 * UNIT, L_Y = RING, Y_P = 96, Y_IM = 64 * Y_P, L_V = L_Y + 2 * Y_IM, V_P = 144, V_IM = 32 * V_P, L_CS = L_V + 2 * V_IM, LDS_BYTES = L_CS + 512;
constexpr int U_X = 1, U_W1 = 17, U_FC = 33, U_W2 = 40, U_G = 68, U_END = 76;
constexpr int NB = 31;
constexpr int c_need(int b) { return b < 4 ? 5 + 4 * b : b < 12 ? 19 + 2 * (b - 4) : b == 12 ? 37 : b < 15 ? 40 : b < 29 ? 42 + 2 * (b - 15) : b == 29 ? 68 : 76; }
constexpr int c_free(int b) { return b == 0 ? 0 : b < 5 ? 1 + 4 * b : b < 13 ? 19 + 2 * (b - 5) : b == 13 ? 37 : b < 16 ? 40 : b < 30 ? 40 + 2 * (b - 15) : 68; }
constexpr int c_min(int a, int b) { return a < b ? a : b; }
constexpr int SPIN = 4000;
constexpr int CODE_PF = 27648;
#define KS_F1 7
#define UN_L3 9
#define UN_D3 9
constexpr int B_POST_F1 = 9 + (KS_F1 - 3) / 4, B_CONF_F1 = 12, B_POST_F2A = 12, B_CONF_F2A = 14, B_POST_F2B = 16, B_CONF_F2B = 15 + (UN_L3 + 1) / 2, B_POST_F3 = 15 + (UN_D3 + 1) / 2, B_CONF_F3 = 29;

#define TS(i) do { } while (0)

template <int N> DI void poll_flags(rsrc_t r, int voff, u32x2 ep) {
#pragma unroll 1
  for (int it = 0; it < SPIN; ++it) {
    bool ok;
    if constexpr (N == 0) { u32x2 v = __builtin_amdgcn_raw_buffer_load_b64(r, voff, 0, 16); ok = v[0] == ep[0] && v[1] == ep[1]; }
    else { ok = true;
#pragma unroll
      for (int k = 0; k < N; ++k) { u32x4 v = __builtin_amdgcn_raw_buffer_load_b128(r, voff + 16 * k, 0, 16); ok = ok && v[0] == ep[0] && v[1] == ep[1] && v[2] == ep[0] && v[3] == ep[1]; } }
    if (__builtin_amdgcn_ballot_w64(!ok) == 0) break;
    __builtin_amdgcn_s_sleep(1);
    asm volatile("" ::: "memory");
  }
  asm volatile("" ::: "memory");
}
DI rsrc_t mk_rsrc(const void* p, int bytes) { return __builtin_amdgcn_make_buffer_rsrc((void*)p, 0, bytes, 0x00020000); }
DI unsigned pf(rsrc_t r, int voff) { return __builtin_amdgcn_raw_buffer_load_b32(r, voff, 0, 0); }
DI void sink(unsigned v) { asm volatile("" ::"v"(v)); }
template <class T> DI T opq(T v) { asm volatile("" : "+v"(v)); return v; }

DI void dma4(rsrc_t r, lptr dst, int v0, int v1, int v2, int v3, int so) {
  __builtin_amdgcn_raw_ptr_buffer_load_lds(r, (LAS void*)dst, 16, v0, so, 0, 0);
  __builtin_amdgcn_raw_ptr_buffer_load_lds(r, (LAS void*)dst, 16, v1, so, 1024, 0);
  __builtin_amdgcn_raw_ptr_buffer_load_lds(r, (LAS void*)dst, 16, v2, so, 2048, 0);
  __builtin_amdgcn_raw_ptr_buffer_load_lds(r, (LAS void*)dst, 16, v3, so, 3072, 0);
}

DI void dma_role(lptr lds, const char* x, const char* w1, const char* w2, int id, int rb, int cg, int cb, int rot, int dw, int lane) {
  const int e = lane >> 3, cp = lane & 7;
  const int vlin = lane * 16;
  const int vx = (lane >> 4) * 32768 + ((lane & 15) ^ (lane >> 4)) * 16;
  const int vch = (((cp >> 1) ^ ((e >> 1) & 1)) * 32) + (cp & 1) * 16;
  const int vw1 = e * 3584 + vch, vw2 = (16 * (e >> 2) + (e & 3)) * 1024 + vch;
  const int x1 = vx ^ 64, x2 = vx ^ 128, x3 = vx ^ 192;
  const int a1 = (vw1 ^ 64) + 27648, a2 = vw1 + 2 * 27648, a3 = (vw1 ^ 64) + 3 * 27648;
  const int b1 = (vw2 ^ 64) + 3072, b2 = vw2 + 2 * 3072, b3 = (vw2 ^ 64) + 3 * 3072;
  const rsrc_t rX = mk_rsrc(x, S_ * D_ * 2), rW1 = mk_rsrc(w1, D_ * DFF * 2), rW2 = mk_rsrc(w2, D_ * DFF * 2);
  const rsrc_t rF = mk_rsrc(&d_FT, 131072), rC = mk_rsrc(&d_FC, 57344), rG = mk_rsrc(&d_GT, 524288);
  const int cgw = cg < 14 ? cg : 13;
  const int sx = (32 * (32 * (rb & 1) + 16 * dw) + 4 * (rb >> 1)) * 1024;
  const int sw1 = 32 * dw * 3584 + cgw * 256, sw2 = 32 * dw * 1024 + cb * 128;
  const int sf = cg * 8192 + dw * 4096, sc = dw * 4096, sg = cb * 65536 + dw * 4096;
  lptr ld = lds + dw * 4096;
  auto issue = [&](auto U) {
    constexpr int u = U;
    lptr dst = ld + (u & 15) * UNIT;
    if constexpr (u < U_X) dma4(rF, dst, vlin, vlin, vlin, vlin, sf);
    else if constexpr (u < U_W1) { constexpr int st = (u - U_X) >> 2, w = (u - U_X) & 3; dma4(rX, dst, vx, x1, x2, x3, sx + w * 131072 + ((st + rot) & 3) * 256); }
    else if constexpr (u < U_FC) { constexpr int q = u - U_W1; dma4(rW1, dst, vw1, a1, a2, a3, sw1 + (((q & 7) + 2 * rot) & 7) * (64 * 3584) + (q >> 3) * 128); }
    else if constexpr (u < U_W2) dma4(rC, dst, vlin, vlin, vlin, vlin, sc + (u - U_FC) * 8192);
    else if constexpr (u < U_G) { constexpr int q = u - U_W2, hb = q >= 14 ? 1 : 0, ci = q - 14 * hb; dma4(rW2, dst, vw2, b1, b2, b3, sw2 + (128 * ci + 64 * hb) * 1024); }
    else dma4(rG, dst, vlin, vlin, vlin, vlin, sg + (u - U_G) * 8192);
  };
  sfor<16>([&](auto U) { issue(U); });
  sfor<NB>([&](auto B) {
    constexpr int b = B;
    constexpr int issued = b == 0 ? 16 : c_min(c_free(b - 1) + 16, U_END);
    constexpr int n = 4 * (issued - c_need(b));
    wait_vm<(n > 63 ? 63 : n)>();
    bar();
    constexpr int hi = c_min(c_free(b) + 16, U_END);
    sfor<hi - issued>([&](auto I) { issue(std::integral_constant<int, issued + I>{}); });
    if constexpr (b == 13) { if (id == 0) { bar(); bar(); bar(); } }
  });
}

DI void sync_role(const char* w1, const char* w2, char* ws, const char* code, int id, int rb, int cg, int n2t, int lane) {
  [[maybe_unused]] const int wid_ts = 1;
  const rsrc_t rs = mk_rsrc(ws, WS_END);
  const int xcd = id & 7, loc = id >> 3;
  TS(0);
  const u32x2 e0 = __builtin_amdgcn_raw_buffer_load_b64(rs, WS_E, 0, 16);
  unsigned p0[4], p1[9];
  { const rsrc_t rI = mk_rsrc(code, CODE_PF), rW1 = mk_rsrc(w1, D_ * DFF * 2), rC = mk_rsrc(&d_FC, 57344);
    p0[0] = pf(rI, ((loc & 3) * 64 + lane) * 128);
    p0[1] = pf(rW1, (16 * loc + (lane >> 3)) * 3584 + (xcd >> 1) * 1024 + (lane & 7) * 128);
    p0[2] = pf(rW1, (16 * loc + 8 + (lane >> 3)) * 3584 + (xcd >> 1) * 1024 + (lane & 7) * 128);
    p0[3] = pf(rC, lane < 14 ? (loc * 14 + lane) * 128 : 0x7ffffff0); }
  u64 E = ((u64)(unsigned)__builtin_amdgcn_readfirstlane(e0[1]) << 32) | (unsigned)__builtin_amdgcn_readfirstlane(e0[0]);
  E = E * 6364136223846793005ull + 1442695040888963407ull;
  const u32x2 ep = {(unsigned)E, (unsigned)(E >> 32)};
  TS(1);
  sfor<NB>([&](auto B) {
    constexpr int b = B;
    if constexpr (b == B_CONF_F1) { poll_flags<0>(rs, WS_F1 + ((id >> 4) * 16 + (lane & 15)) * 8, ep); TS(3); }
    if constexpr (b == B_CONF_F2A) { poll_flags<0>(rs, WS_F2 + ((2 * (n2t >> 2)) * 16 + (lane & 31)) * 8, ep); TS(5); }
    if constexpr (b == B_CONF_F2B) { poll_flags<0>(rs, WS_F2 + 2048 + ((2 * (n2t >> 2)) * 16 + (lane & 31)) * 8, ep); TS(7); }
    if constexpr (b == B_CONF_F3) { poll_flags<2>(rs, WS_F3 + lane * 32, ep); TS(9); }
    bar();
    if constexpr (b == B_POST_F1) { if (lane == 0) st_flag(ep, rs, WS_F1 + (cg * 16 + rb) * 8); TS(2); }
    if constexpr (b == B_POST_F2A) { if (lane == 0) st_flag(ep, rs, WS_F2 + (rb * 16 + cg) * 8); TS(4); }
    if constexpr (b == B_POST_F2B) { if (lane == 0) st_flag(ep, rs, WS_F2 + 2048 + (rb * 16 + cg) * 8); TS(6); }
    if constexpr (b == B_POST_F3) { if (lane == 0) st_flag(ep, rs, WS_F3 + id * 8); TS(8); }
    if constexpr (b == B_CONF_F3) { if (id == 0 && lane == 0) st_flag(ep, rs, WS_E); }
    if constexpr (b == 1) {
      const rsrc_t rW2 = mk_rsrc(w2, D_ * DFF * 2), rG = mk_rsrc(&d_GT, 524288);
#pragma unroll
      for (int i = 0; i < 4; ++i) sink(p0[i]);
#pragma unroll
      for (int i = 0; i < 7; ++i) p1[i] = pf(rW2, loc * 57344 + (i * 64 + lane) * 128);
#pragma unroll
      for (int i = 0; i < 2; ++i) p1[7 + i] = pf(rG, loc * 16384 + (i * 64 + lane) * 128);
    }
    if constexpr (b == 8) {
#pragma unroll
      for (int i = 0; i < 9; ++i) sink(p1[i]);
    }
    if constexpr (b == 13) { if (id == 0) { bar(); bar(); bar(); } }
  });
}

DI void compute_role(lptr lds, const uint16_t* __restrict__ x, const uint16_t* __restrict__ wr, const uint16_t* __restrict__ wi, char* ws, uint16_t* __restrict__ out,
                     int id, int rb, int cg, int n2t, int cb, int rot, int w, int lane) {
  int g = lane >> 4, li = lane & 15;
  auto fresh = [&]() { lane = opq(lane); g = lane >> 4; li = lane & 15; };
  [[maybe_unused]] const int wid_ts = w == 0 ? 0 : -1;
  const rsrc_t rs = mk_rsrc(ws, WS_END);
  TS(0);
  const int wk = rb >> 2, gk = rb & 3;
  uint16_t wa[8], wb[8];
  f32x4 uv0 = {0.f, 0.f, 0.f, 0.f};
  { const int k1 = 16 * wk + 4 * w + g, j = 16 * cg + li;
#pragma unroll
    for (int e = 0; e < 8; ++e) { const int k = k1 + 64 * (16 * (e >> 2) + 4 * gk + (e & 3)); wa[e] = wr[k * 257 + j]; wb[e] = wi[k * 257 + j]; }
    if (cg == 0 && li < 8) {
      const int k = k1 + 64 * (16 * (li >> 2) + 4 * gk + (li & 3)), km = (2048 - k) & 2047;
      const float w0r = bflo(wr[k * 257]), w0i = bflo(wi[k * 257]), m0r = bflo(wr[km * 257]), m0i = bflo(wi[km * 257]);
      const float wnr = bflo(wr[k * 257 + 256]), wni = bflo(wi[k * 257 + 256]), mnr = bflo(wr[km * 257 + 256]), mni = bflo(wi[km * 257 + 256]);
      const float ar = 0.5f * (w0r + m0r), ai = 0.5f * (w0i - m0i), br = 0.5f * (wnr + mnr), bi = 0.5f * (wni - mni);
      uv0 = f32x4{0.25f * (ar + br), 0.25f * (ai + bi), 0.25f * (ar - br), 0.25f * (ai - bi)};
    } }
  bf16x8 xr[2][16];
  f32x4 acc[2][4];
  const f32x4 z4 = {0.f, 0.f, 0.f, 0.f};
  float sum[2] = {0.f, 0.f}, sq[2] = {0.f, 0.f};
#pragma unroll
  for (int m = 0; m < 2; ++m) { acc[m][0] = z4; acc[m][1] = z4; }
  const int xa = li * 256 + ((g ^ li) * 16);
  bf16x8 ff[2][4];
  sfor<4>([&](auto ST) {
    constexpr int st = ST;
    bar();
    if constexpr (st == 0) TS(1);
    const int xs = ((U_X + 4 * st + w) & 15) * UNIT;
    if constexpr (st == 0) {
#pragma unroll
      for (int k = 0; k < 4; ++k) { ff[0][k] = lds_b128(lds + lane * 16 + k * 1024); ff[1][k] = lds_b128(lds + lane * 16 + 4096 + k * 1024); }
    }
#pragma unroll
    for (int k = 0; k < 4; ++k) { xr[0][4 * st + k] = lds_b128(lds + xs + (xa ^ (k << 6))); xr[1][4 * st + k] = lds_b128(lds + xs + (xa ^ (k << 6)) + 4096); }
    f32x4 C[2] = {z4, z4}, Sn[2] = {z4, z4};
#pragma unroll
    for (int k = 0; k < 4; ++k)
#pragma unroll
      for (int m = 0; m < 2; ++m) {
        C[m] = mfma(xr[m][4 * st + k], ff[0][k], C[m]); Sn[m] = mfma(xr[m][4 * st + k], ff[1][k], Sn[m]);
        const u32x4 a = __builtin_bit_cast(u32x4, xr[m][4 * st + k]);
#pragma unroll
        for (int e = 0; e < 4; ++e) { sum[m] = dot2(a[e], 0x3f803f80u, sum[m]); sq[m] = dot2(a[e], a[e], sq[m]); }
      }
    const int q = ((li & 3) * ((st + rot) & 3)) & 3;
    const bool sw = q & 1;
    const float sr = q < 2 ? 1.f : -1.f, si = (q == 0 || q == 3) ? 1.f : -1.f;
#pragma unroll
    for (int m = 0; m < 2; ++m)
#pragma unroll
      for (int r = 0; r < 4; ++r) {
        acc[m][0][r] += sr * (sw ? Sn[m][r] : C[m][r]); acc[m][1][r] += si * (sw ? C[m][r] : Sn[m][r]);
      }
    if constexpr (st == 2) {
      const int wo = WS_WT + (16 * cg + li) * 8192 + wk * 2048 + (16 * gk + 4 * w + g) * 16;
      unsigned wv[8];
#pragma unroll
      for (int e = 0; e < 8; ++e) wv[e] = (unsigned)wa[e] | ((unsigned)wb[e] << 16);
      st16(u32x4{wv[0], wv[1], wv[2], wv[3]}, rs, wo, 0);
      st16(u32x4{wv[4], wv[5], wv[6], wv[7]}, rs, wo + 1024, 0);
      if (cg == 0 && li < 8) st16(__builtin_bit_cast(u32x4, uv0), rs, WS_WS + ((wk * 8 + li) * 64 + 16 * gk + 4 * w + g) * 16, 0);
    }
    wait_lgkm0();
  });
  fresh();
  int ta = (8 * g + (li >> 2)) * 128 + (li & 3) * 8 + ((((li >> 3) & 1) + 2 * (g & 1)) * 32);
  bf16x8 wf[2][4];
  int sb = (U_W1 & 15) * UNIT;
  auto rd2 = [&](int buf, int base, int ksl) {
#pragma unroll
    for (int t = 0; t < 4; ++t) wf[buf][t] = lds_tr2(lds + base + ((ta ^ (w << 5)) ^ (t << 5)) + ksl * 4096);
  };
  bar();
  TS(2);
  rd2(0, sb, 0);
  sbar();
  float mu[2], rsd[2];
  {
#pragma unroll
    for (int m = 0; m < 2; ++m) {
      const float s1 = sum_g(sum[m]), s2 = sum_g(sq[m]);
      const float mean = s1 * (1.f / 512.f);
      const float var = fmaxf(s2 * (1.f / 512.f) - mean * mean, 0.f);
      const float den = bf_round(__builtin_amdgcn_sqrtf(bf_round(bf_round(var) + 1e-5f)));
      mu[m] = bf_round(mean); rsd[m] = __builtin_amdgcn_rcpf(den);
    }
    f32x4 v[2][2];
#pragma unroll
    for (int m = 0; m < 2; ++m) {
      f32x4 r4, m4;
#pragma unroll
      for (int r = 0; r < 4; ++r) { r4[r] = bperm(rsd[m], 4 * g + r); m4[r] = bperm(mu[m], 4 * g + r); }
      const float csf = (cg == 0 && li == 0) ? 512.f : 0.f;
      v[m][0] = (acc[m][0] - m4 * csf) * r4; v[m][1] = acc[m][1] * r4;
    }
    const int ao = WS_AT + (16 * cg + li) * 8192 + (rb >> 1) * 512 + (rb & 1) * 256 + w * 64 + g * 16;
    st16(__builtin_bit_cast(u32x4, pack8(v[0][0], v[1][0])), rs, ao, 0);
    st16(__builtin_bit_cast(u32x4, pack8(v[0][1], v[1][1])), rs, ao + 4096, 0);
  }
  TS(3);
  const int j = id, pu = w & 1, pv = w >> 1;
  auto h_epilogue = [&](int hb) {
    fresh();
    const float mu_[2] = {opq(mu[0]), opq(mu[1])}, rsd_[2] = {opq(rsd[0]), opq(rsd[1])};
    const bool odd = w & 1;
    const int hoff = cg < 14 ? WS_H + (((4 * (rb >> 1) + (li >> 2)) * 4 + 2 * (rb & 1)) * 56) * 1024 + (16 * g + 4 * w + (li & 3)) * 16 : 0x7f000000;
#pragma unroll
    for (int m = 0; m < 2; ++m)
#pragma unroll
      for (int u = 0; u < 2; ++u) {
        f32x4 v[2];
#pragma unroll
        for (int t = 0; t < 2; ++t) {
          const f32x4 c4 = lds_f4(lds + L_CS + hb * 256 + (((2 * u + t) ^ w) * 64) + g * 16);
          v[t] = (acc[m][2 * u + t] - c4 * mu_[m]) * rsd_[m];
#pragma unroll
          for (int e = 0; e < 4; ++e) v[t][e] = fmaxf(v[t][e], 0.f);
        }
        const u32x2 p0 = __builtin_bit_cast(u32x2, pack4(v[0])), p1 = __builtin_bit_cast(u32x2, pack4(v[1]));
        const u32x4 o = {odd ? p1[0] : p0[0], odd ? p1[1] : p0[1], odd ? p0[0] : p1[0], odd ? p0[1] : p1[1]};
        const int ks56 = hb * 28 + 2 * cg + (u ^ (w >> 1));
        st16(o, rs, hoff + m * 56 * 1024, ks56 * 1024);
      }
  };
#pragma unroll 1
  for (int hb = 0;; ++hb) {
#pragma unroll
    for (int m = 0; m < 2; ++m)
#pragma unroll
      for (int t = 0; t < 4; ++t) acc[m][t] = z4;
    float cs = 0.f;
    sfor<16>([&](auto KS) {
      constexpr int ks = KS, cur = ks & 1, nxt = cur ^ 1;
      sbar();
      if constexpr (ks < 15) {
        if constexpr ((ks & 3) == 3) {
          wait_lgkm0();
          if constexpr (ks == KS_F1) { if (hb == 1) wait_vm<4>(); }
          bar();
        }
        if constexpr (((ks + 1) & 1) == 0) sb = (sb + UNIT) & (RING - 1);
        rd2(nxt, sb, (ks + 1) & 1);
      }
#pragma unroll
      for (int t = 0; t < 4; ++t)
#pragma unroll
        for (int m = 0; m < 2; ++m) acc[m][t] = mfma(wf[cur][t], xr[m][ks], acc[m][t]);
      { const u32x4 a = __builtin_bit_cast(u32x4, wf[cur][0]);
#pragma unroll
        for (int e = 0; e < 4; ++e) cs = dot2(a[e], 0x3f803f80u, cs); }
      if constexpr (ks < 15) {
#pragma unroll
        for (int i = 0; i < 8; ++i) { __builtin_amdgcn_sched_group_barrier(0x008, 1, 0); __builtin_amdgcn_sched_group_barrier(0x100, 1, 0); }
      }
    });
    sbar();
    cs = sum_g(cs);
    if (lane < 16) *(LAS float*)(lds + L_CS + hb * 256 + w * 64 + lane * 4) = cs;
    wait_lgkm0();
    if (hb == 1) break;
    bar();
    sb = (sb + UNIT) & (RING - 1);
    rd2(0, sb, 0);
    sbar();
    h_epilogue(0);
    sbar();
    TS(4);
  }
  wait_vm<0>();
  bar();
  fresh();
  bf16x8 zr[2], zi[2];
  u32x4 wq[2];
  { const int zo = WS_AT + j * 8192 + (4 * pu + (li >> 2)) * 512 + g * 64 + (li & 3) * 16;
#pragma unroll
    for (int ks = 0; ks < 2; ++ks) { zr[ks] = __builtin_bit_cast(bf16x8, ld16(rs, zo + ks * 256, 0)); zi[ks] = __builtin_bit_cast(bf16x8, ld16(rs, zo + 4096 + ks * 256, 0)); }
    wq[0] = ld16(rs, WS_WT + j * 8192 + w * 2048 + lane * 16, 0); wq[1] = ld16(rs, WS_WT + j * 8192 + w * 2048 + 1024 + lane * 16, 0);
  }
  sbar();
  h_epilogue(1);
  sbar();
  TS(5);
  bf16x8 hr[28];
  fresh();
  const int ho = WS_H + (n2t * 4 + w) * 56 * 1024 + lane * 16;
  {
    lptr fc = lds + (U_FC & 15) * UNIT + lane * 16;
    bf16x8 c64[2][2][2];
    {
      f32x4 tw1[2][2];
#pragma unroll
      for (int t = 0; t < 2; ++t)
#pragma unroll
        for (int p = 0; p < 2; ++p) {
#pragma unroll
          for (int ks = 0; ks < 2; ++ks) c64[t][ks][p] = lds_b128(fc + pv * 8192 + ((t * 2 + ks) * 2 + p) * 1024);
          tw1[t][p] = lds_f4(fc + 16384 + w * 4096 + (t * 2 + p) * 1024);
        }
#pragma unroll
      for (int t = 0; t < 2; ++t) {
        f32x4 yr = z4, yi = z4;
#pragma unroll
        for (int ks = 0; ks < 2; ++ks) {
          yr = mfma(zr[ks], c64[t][ks][0], yr); yr = mfma(neg8(zi[ks]), c64[t][ks][1], yr);
          yi = mfma(zi[ks], c64[t][ks][0], yi); yi = mfma(zr[ks], c64[t][ks][1], yi);
        }
        const f32x4 pr = yr * tw1[t][0] + yi * tw1[t][1], pi = yi * tw1[t][0] - yr * tw1[t][1];
        lptr p = lds + L_Y + (32 * pv + 8 * (li >> 2) + 4 * t + (li & 3)) * Y_P + (8 * g + 4 * pu) * 2;
        *(LAS bf16x4*)p = pack4(pr); *(LAS bf16x4*)(p + Y_IM) = pack4(pi);
      }
    }
    wait_lgkm0();
    bar();
    TS(6);
    bf16x8 c32[2][2];
    f32x4 tw3[2][2];
#pragma unroll
    for (int t = 0; t < 2; ++t)
#pragma unroll
      for (int p = 0; p < 2; ++p) { c32[t][p] = lds_b128(fc + 32768 + (t * 2 + p) * 1024); tw3[t][p] = lds_f4(fc + 40960 + w * 4096 + (t * 2 + p) * 1024); }
    f32x4 xre[2], xim[2];
    {
      lptr p = lds + L_Y + (16 * w + li) * Y_P + g * 16;
      const bf16x8 ur = lds_b128(p), ui = lds_b128(p + Y_IM), nui = neg8(ui);
#pragma unroll
      for (int t = 0; t < 2; ++t) {
        xre[t] = mfma(c32[t][0], ur, z4); xre[t] = mfma(c32[t][1], nui, xre[t]);
        xim[t] = mfma(c32[t][1], ur, z4); xim[t] = mfma(c32[t][0], ui, xim[t]);
      }
    }
    f32x4 pr[2], pi[2];
    if (j != 0) {
#pragma unroll
      for (int t = 0; t < 2; ++t)
#pragma unroll
        for (int r = 0; r < 4; ++r) {
          const float wre = bflo(wq[t][r]), wim = bfhi(wq[t][r]);
          pr[t][r] = xre[t][r] * wre - xim[t][r] * wim; pi[t][r] = xre[t][r] * wim + xim[t][r] * wre;
        }
    } else {
      wait_lgkm0();
      bar();
      LAS f32x2* X = (LAS f32x2*)(lds + L_Y);
#pragma unroll
      for (int t = 0; t < 2; ++t)
#pragma unroll
        for (int r = 0; r < 4; ++r) X[16 * w + li + 64 * (16 * t + 4 * g + r)] = f32x2{xre[t][r], xim[t][r]};
      wait_lgkm0();
      bar();
#pragma unroll
      for (int t = 0; t < 2; ++t)
#pragma unroll
        for (int r = 0; r < 4; ++r) {
          const int k = 16 * w + li + 64 * (16 * t + 4 * g + r);
          const f32x2 xm = X[(2048 - k) & 2047];
          const f32x4 c = __builtin_bit_cast(f32x4, ld16(rs, WS_WS + ((w * 8 + 4 * t + r) * 64 + lane) * 16, 0));
          pr[t][r] = xre[t][r] * c[0] - xim[t][r] * c[1] + xm[0] * c[2] + xm[1] * c[3];
          pi[t][r] = xre[t][r] * c[1] + xim[t][r] * c[0] + xm[0] * c[3] - xm[1] * c[2];
        }
      wait_lgkm0();
      bar();
    }
    {
      const bf16x8 Pr = pack8(pr[0], pr[1]), Pi = pack8(pi[0], pi[1]), nPr = neg8(Pr);
#pragma unroll
      for (int t = 0; t < 2; ++t) {
        f32x4 vr = mfma(Pr, c32[t][0], z4); vr = mfma(Pi, c32[t][1], vr);
        f32x4 vi = mfma(Pi, c32[t][0], z4); vi = mfma(nPr, c32[t][1], vi);
        const f32x4 ar = vr * tw3[t][0] - vi * tw3[t][1], ai = vr * tw3[t][1] + vi * tw3[t][0];
        lptr p = lds + L_V + (16 * t + li) * V_P + (32 * (w >> 1) + 8 * g + 4 * (w & 1)) * 2;
        *(LAS bf16x4*)p = pack4(ar); *(LAS bf16x4*)(p + V_IM) = pack4(ai);
      }
    }
    wait_lgkm0();
    bar();
    sbar();
#pragma unroll
    for (int i = 0; i < 28; ++i) hr[i] = __builtin_bit_cast(bf16x8, ld16(rs, ho, i * 1024));
    sbar();
    {
      bf16x8 vr[2], vi[2];
#pragma unroll
      for (int ks = 0; ks < 2; ++ks) { lptr p = lds + L_V + (16 * pu + li) * V_P + (32 * ks + 8 * g) * 2; vr[ks] = lds_b128(p); vi[ks] = lds_b128(p + V_IM); }
      f32x4 yr[2], yi[2];
#pragma unroll
      for (int t = 0; t < 2; ++t) {
        yr[t] = z4; yi[t] = z4;
#pragma unroll
        for (int ks = 0; ks < 2; ++ks) {
          yr[t] = mfma(c64[t][ks][0], vr[ks], yr[t]); yr[t] = mfma(c64[t][ks][1], vi[ks], yr[t]);
          yi[t] = mfma(c64[t][ks][0], vi[ks], yi[t]); yi[t] = mfma(c64[t][ks][1], neg8(vr[ks]), yi[t]);
        }
      }
      const int cre = 32 * (j >> 4) + (j & 15);
      const int bo = WS_BT + cre * 4096 + (16 * pu + li) * 128 + (32 * pv + 8 * g) * 2;
      st16(__builtin_bit_cast(u32x4, pack8(yr[0], yr[1])), rs, bo, 0);
      st16(__builtin_bit_cast(u32x4, pack8(yi[0], yi[1])), rs, bo + 16 * 4096, 0);
    }
    TS(7);
  }
  f32x4 acc1[4], acc2[4];
  u32x2 xv[4];
  {
    fresh();
    ta = (8 * g + (li >> 2)) * 128 + (li & 3) * 8 + ((((li >> 3) & 1) + 2 * (g & 1)) * 32);
    wait_lgkm0();
    bar();
    TS(8);
    bf16x8 wg[2][8];
    sb = (U_W2 & 15) * UNIT;
    auto rd4 = [&](int buf, int base) {
#pragma unroll
      for (int k = 0; k < 2; ++k)
#pragma unroll
        for (int t = 0; t < 4; ++t) wg[buf][k * 4 + t] = lds_tr2(lds + base + (ta ^ (t << 5)) + k * 4096);
    };
    rd4(0, sb);
#pragma unroll
    for (int t = 0; t < 4; ++t) acc1[t] = z4;
#pragma unroll 1
    for (int it = 0; it < 2; ++it) {
      const int hn = it == 0 ? ho + 28 * 1024 : 0x7f000000;
      sfor<14>([&](auto UN) {
        constexpr int un = UN, cur = un & 1, nxt = cur ^ 1;
        sbar();
        if constexpr (un & 1) {
          wait_lgkm0();
          if constexpr (un == 1) { if (it == 0) wait_vm<30>(); }
          if constexpr (un == UN_D3) { if (it == 0) wait_vm<(UN_D3 > UN_L3 ? 2 * UN_D3 : 0)>(); }
          bar();
          if constexpr (un == UN_L3) {
#pragma unroll
            for (int i = 0; i < 2 * un; ++i) hr[i] = __builtin_bit_cast(bf16x8, ld16(rs, hn, i * 1024));
            sbar();
          }
        }
        sb = (sb + UNIT) & (RING - 1);
        if (!(un == 13 && it == 1)) rd4(nxt, sb);
#pragma unroll
        for (int k = 0; k < 2; ++k)
#pragma unroll
          for (int t = 0; t < 4; ++t) acc1[t] = mfma(wg[cur][k * 4 + t], hr[2 * un + k], acc1[t]);
        if constexpr (un >= UN_L3) { hr[2 * un] = __builtin_bit_cast(bf16x8, ld16(rs, hn, (2 * un) * 1024)); hr[2 * un + 1] = __builtin_bit_cast(bf16x8, ld16(rs, hn, (2 * un + 1) * 1024)); }
#pragma unroll
        for (int i = 0; i < 8; ++i) { __builtin_amdgcn_sched_group_barrier(0x008, 1, 0); __builtin_amdgcn_sched_group_barrier(0x100, 2, 0); }
      });
    }
    sbar();
    TS(9);
  }
  fresh();
  ta = (8 * g + (li >> 2)) * 128 + (li & 3) * 8 + ((((li >> 3) & 1) + 2 * (g & 1)) * 32);
  const int nrow = 32 * (16 * w + li) + n2t;
  {
    u32x4 bq[16];
    const int e8 = lane >> 3, cp = lane & 7;
    const int bo = WS_BT + (16 * w + e8) * 4096 + n2t * 128 + cp * 16;
#pragma unroll
    for (int b = 0; b < 8; ++b) { bq[2 * b] = ld16(rs, bo, b * 64 * 4096); bq[2 * b + 1] = ld16(rs, bo, b * 64 * 4096 + 8 * 4096); }
#pragma unroll
    for (int t = 0; t < 4; ++t) xv[t] = *(const u32x2*)(x + (size_t)nrow * 512 + 64 * cb + 16 * t + 4 * g);
    sbar();
#pragma unroll
    for (int t = 0; t < 4; ++t)
#pragma unroll
      for (int r = 0; r < 4; ++r) { const float v = bf_round(acc1[t][r]); acc1[t][r] = bf_round(v * __builtin_amdgcn_rcpf(1.f + __expf(-v))); }
    sbar();
    const int lo = (16 * w + e8) * 128 + (((cp >> 1) ^ ((e8 >> 1) & 1)) * 32) + (cp & 1) * 16;
#pragma unroll
    for (int b = 0; b < 8; ++b) {
      lptr p = lds + ((U_END + b) & 15) * UNIT;
      *(LAS u32x4*)(p + lo) = bq[2 * b]; *(LAS u32x4*)(p + ((lo + 1024) ^ 64)) = bq[2 * b + 1];
    }
    wait_lgkm0();
    TS(10);
    bar();
    TS(11);
#pragma unroll
    for (int t = 0; t < 4; ++t) acc2[t] = z4;
#pragma unroll
    for (int b = 0; b < 8; ++b) {
      lptr gs = lds + ((U_G + b) & 15) * UNIT + lane * 16;
      lptr bs = lds + ((U_END + b) & 15) * UNIT + (ta ^ (w << 5));
      bf16x8 gf[8], bf[2];
#pragma unroll
      for (int k = 0; k < 2; ++k) {
        bf[k] = lds_tr2(bs + k * 4096);
#pragma unroll
        for (int t = 0; t < 4; ++t) gf[k * 4 + t] = lds_b128(gs + (t * 2 + k) * 1024);
      }
#pragma unroll
      for (int k = 0; k < 2; ++k)
#pragma unroll
        for (int t = 0; t < 4; ++t) acc2[t] = mfma(gf[k * 4 + t], bf[k], acc2[t]);
    }
  }
  TS(12);
  u32x2 o[4];
#pragma unroll
  for (int t = 0; t < 4; ++t) {
    f32x4 r4;
#pragma unroll
    for (int r = 0; r < 4; ++r) {
      const float sv = bf_round(acc2[t][r] * (1.f / 524288.f));
      const float xin = (r & 1) ? bfhi(xv[t][r >> 1]) : bflo(xv[t][r >> 1]);
      r4[r] = xin + bf_round(sv * acc1[t][r]);
    }
    o[t] = __builtin_bit_cast(u32x2, pack4(r4));
  }
#pragma unroll
  for (int u = 0; u < 2; ++u) {
    auto s0 = __builtin_amdgcn_permlane16_swap(o[2 * u][0], o[2 * u + 1][0], false, false);
    auto s1 = __builtin_amdgcn_permlane16_swap(o[2 * u][1], o[2 * u + 1][1], false, false);
    const u32x4 q = {(unsigned)s0[0], (unsigned)s1[0], (unsigned)s0[1], (unsigned)s1[1]};
    *(u32x4*)(out + (size_t)nrow * 512 + 64 * cb + 32 * u + 16 * (g & 1) + 8 * (g >> 1)) = q;
  }
  TS(13);
}

__global__ __launch_bounds__(448, 2) void fused_k(const uint16_t* __restrict__ x, const uint16_t* __restrict__ wr, const uint16_t* __restrict__ wi,
                                                   const uint16_t* __restrict__ w1, const uint16_t* __restrict__ w2, char* __restrict__ ws, uint16_t* __restrict__ out) {
  extern __shared__ __attribute__((aligned(16))) char smem[];
  lptr lds = (lptr)smem;
  u64 pc0;
  asm volatile("s_getpc_b64 %0" : "=s"(pc0));
  const char* code = (const char*)(pc0 & ~(u64)127);
  const int wid = __builtin_amdgcn_readfirstlane(threadIdx.x >> 6), lane = threadIdx.x & 63;
  const int id = blockIdx.x, xcd = id & 7, loc = id >> 3;
  const int rb = 8 * (xcd & 1) + (loc & 7), cg = 4 * (xcd >> 1) + (loc >> 3);
  const int n2t = 4 * xcd + (loc & 3), cb = loc >> 2;
  const int rot = ((loc >> 3) + loc) & 3;
  if (wid >= 6) sync_role((const char*)w1, (const char*)w2, ws, code, id, rb, cg, n2t, lane);
  else if (wid >= 4) dma_role(lds, (const char*)x, (const char*)w1, (const char*)w2, id, rb, cg, cb, rot, wid - 4, lane);
  else compute_role(lds, x, wr, wi, ws, out, id, rb, cg, n2t, cb, rot, wid, lane);
}

extern "C" void kernel_launch(void* const* d_in, const int* in_sizes, int n_in, void* d_out, int out_size, void* d_ws, size_t ws_size, hipStream_t stream) {
  hipFuncSetAttribute((const void*)fused_k, hipFuncAttributeMaxDynamicSharedMemorySize, LDS_BYTES);
  fused_k<<<256, 448, LDS_BYTES, stream>>>((const uint16_t*)d_in[0], (const uint16_t*)d_in[1], (const uint16_t*)d_in[2], (const uint16_t*)d_in[4], (const uint16_t*)d_in[5],
                                           (char*)d_ws, (uint16_t*)d_out);
}
```

```cpp
#include <hip/hip_runtime.h>
#include <stdint.h>
#include <utility>

#define DI __device__ __forceinline__
typedef __attribute__((ext_vector_type(8))) __bf16 bf16x8;
typedef __attribute__((ext_vector_type(4))) __bf16 bf16x4;
typedef __attribute__((ext_vector_type(2))) __bf16 bf16x2;
typedef __attribute__((ext_vector_type(4))) float f32x4;
typedef __attribute__((ext_vector_type(2))) float f32x2;
typedef __attribute__((ext_vector_type(4))) short s16x4;
typedef __attribute__((ext_vector_type(8))) short s16x8;
typedef __attribute__((ext_vector_type(4))) unsigned u32x4;
typedef __attribute__((ext_vector_type(2))) unsigned u32x2;
typedef unsigned long long u64;
#define LAS __attribute__((address_space(3)))
typedef LAS char* lptr;
typedef __amdgpu_buffer_rsrc_t rsrc_t;

template <class F, int... I> DI void sfor_impl(F&& f, std::integer_sequence<int, I...>) { (f(std::integral_constant<int, I>{}), ...); }
template <int N, class F> DI void sfor(F&& f) { sfor_impl(f, std::make_integer_sequence<int, N>{}); }

constexpr double kPi = 3.14159265358979323846264338327950288;
constexpr double c_cos(double x) { double x2 = x * x, term = 1.0, sum = 1.0; for (int i = 1; i < 18; ++i) { term *= -x2 / ((2 * i - 1) * (2 * i)); sum += term; } return sum; }
constexpr uint16_t c_f2bf(float f) { uint32_t u = __builtin_bit_cast(uint32_t, f); u += 0x7fffu + ((u >> 16) & 1u); return (uint16_t)(u >> 16); }
struct Cos2048 { float v[2048]; constexpr Cos2048() : v{} { for (int m = 0; m < 2048; ++m) { int mm = m <= 1024 ? m : 2048 - m; double c = c_cos(2.0 * kPi * mm / 2048.0); if (mm == 512) c = 0.0; v[m] = (float)c; } } };
constexpr Cos2048 C2K{};
struct Cos512 { uint16_t v[512]; constexpr Cos512() : v{} { for (int m = 0; m < 512; ++m) v[m] = c_f2bf(C2K.v[4 * m]); } };
constexpr Cos512 C512{};
constexpr uint16_t fval(int d, int c) { const int T = c >> 4, j = 16 * (T >> 1) + (c & 15); const int m = (T & 1) == 0 ? ((d * j) & 511) : (j == 0 ? (d & 1) * 256 : ((d * j + 128) & 511)); return C512.v[m]; }
constexpr int c_sl(int g, int e) { return 16 * (e >> 2) + 4 * g + (e & 3); }
constexpr int c_rho(int v, int t, int li) { return 32 * v + 8 * (li >> 2) + 4 * t + (li & 3); }

template <int P> struct FPart { uint16_t v[16384];
  constexpr FPart() : v{} { for (int cq = 0; cq < 4; ++cq) for (int t = 0; t < 2; ++t) for (int k = 0; k < 4; ++k) for (int l = 0; l < 64; ++l) for (int e = 0; e < 8; ++e)
    v[(((cq * 2 + t) * 4 + k) * 64 + l) * 8 + e] = fval(32 * k + 8 * (l >> 4) + e, 32 * (4 * P + cq) + 16 * t + (l & 15)); } };
template <int P> struct GPart { uint16_t v[16384];
  constexpr GPart() : v{} { for (int s = 0; s < 4; ++s) for (int t = 0; t < 4; ++t) for (int k = 0; k < 2; ++k) for (int l = 0; l < 64; ++l) for (int e = 0; e < 8; ++e)
    v[(((s * 4 + t) * 2 + k) * 64 + l) * 8 + e] = fval(64 * (P >> 1) + 16 * t + (l & 15), 64 * (4 * (P & 1) + s) + 32 * k + 8 * (l >> 4) + e); } };
struct FFTC { uint16_t c64[8192]; float tw1[4096]; uint16_t c32[4096]; float tw3[4096];
  constexpr FFTC() : c64{}, tw1{}, c32{}, tw3{} {
    for (int l = 0; l < 64; ++l) { const int g = l >> 4, li = l & 15;
      for (int p = 0; p < 2; ++p) {
        for (int e = 0; e < 8; ++e) {
          for (int v = 0; v < 2; ++v) for (int t = 0; t < 2; ++t) for (int ks = 0; ks < 2; ++ks)
            c64[((((v * 2 + t) * 2 + ks) * 2 + p) * 64 + l) * 8 + e] = C512.v[(8 * c_rho(v, t, li) * (32 * ks + c_sl(g, e)) + 128 * p) & 511];
          for (int t = 0; t < 2; ++t) c32[((t * 2 + p) * 64 + l) * 8 + e] = C512.v[(16 * (16 * t + li) * c_sl(g, e) + 128 * p) & 511];
        }
        for (int w = 0; w < 4; ++w) for (int t = 0; t < 2; ++t) for (int r = 0; r < 4; ++r) {
          tw1[(((w * 2 + t) * 2 + p) * 64 + l) * 4 + r] = C2K.v[((16 * (w & 1) + 4 * g + r) * c_rho(w >> 1, t, li) + 1536 * p) & 2047];
          tw3[(((w * 2 + t) * 2 + p) * 64 + l) * 4 + r] = C2K.v[((16 * w + 4 * g + r) * (16 * t + li) + 1536 * p) & 2047];
        } } } } };
struct FBig { FPart<0> a0; FPart<1> a1; FPart<2> a2; FPart<3> a3; };
struct GBig { GPart<0> a0; GPart<1> a1; GPart<2> a2; GPart<3> a3; GPart<4> a4; GPart<5> a5; GPart<6> a6; GPart<7> a7; GPart<8> a8; GPart<9> a9; GPart<10> a10; GPart<11> a11; GPart<12> a12; GPart<13> a13; GPart<14> a14; GPart<15> a15; };
#ifdef __HIP_DEVICE_COMPILE__
#define FP(n) constexpr FPart<n> fp##n{};
FP(0) FP(1) FP(2) FP(3)
#define GP(n) constexpr GPart<n> gp##n{};
GP(0) GP(1) GP(2) GP(3) GP(4) GP(5) GP(6) GP(7) GP(8) GP(9) GP(10) GP(11) GP(12) GP(13) GP(14) GP(15)
__device__ const FBig d_FT = {fp0, fp1, fp2, fp3};
__device__ const GBig d_GT = {gp0, gp1, gp2, gp3, gp4, gp5, gp6, gp7, gp8, gp9, gp10, gp11, gp12, gp13, gp14, gp15};
__device__ const FFTC d_FC = FFTC();
#else
__device__ const uint16_t d_FT[4 * 16384] = {0};
__device__ const uint16_t d_GT[16 * 16384] = {0};
__device__ const uint16_t d_FC[28672] = {0};
#endif

DI f32x4 mfma(bf16x8 a, bf16x8 b, f32x4 c) { return __builtin_amdgcn_mfma_f32_16x16x32_bf16(a, b, c, 0, 0, 0); }
DI bf16x8 lds_b128(lptr p) { return *(LAS bf16x8*)p; }
DI f32x4 lds_f4(lptr p) { return *(LAS f32x4*)p; }
DI s16x4 lds_tr(lptr p) { return __builtin_amdgcn_ds_read_tr16_b64_v4i16((LAS s16x4*)p); }
DI bf16x8 lds_tr2(lptr p) { s16x4 a = lds_tr(p), b = lds_tr(p + 512); s16x8 v = __builtin_shufflevector(a, b, 0, 1, 2, 3, 4, 5, 6, 7); return __builtin_bit_cast(bf16x8, v); }
DI void glds16(const void* g, lptr l) { __builtin_amdgcn_global_load_lds((const unsigned*)g, (LAS unsigned*)l, 16, 0, 0); }
template <int N> DI void wait_vm() { asm volatile("s_waitcnt vmcnt(%0)" ::"n"(N) : "memory"); }
DI void wait_lgkm0() { __builtin_amdgcn_s_waitcnt(0xc07f); }
DI void sbar() { __builtin_amdgcn_sched_barrier(0); }
DI void bar() { asm volatile("" ::: "memory"); sbar(); __builtin_amdgcn_s_barrier(); sbar(); asm volatile("" ::: "memory"); }
DI float dot2(unsigned a, unsigned b, float c) { return __builtin_amdgcn_fdot2_f32_bf16(__builtin_bit_cast(bf16x2, a), __builtin_bit_cast(bf16x2, b), c, false); }
DI float bf_round(float f) { return (float)(__bf16)f; }
DI float bflo(unsigned u) { return __builtin_bit_cast(float, u << 16); }
DI float bfhi(unsigned u) { return __builtin_bit_cast(float, u & 0xffff0000u); }
DI bf16x8 pack8(f32x4 a, f32x4 b) { bf16x8 r; r[0] = (__bf16)a[0]; r[1] = (__bf16)a[1]; r[2] = (__bf16)a[2]; r[3] = (__bf16)a[3]; r[4] = (__bf16)b[0]; r[5] = (__bf16)b[1]; r[6] = (__bf16)b[2]; r[7] = (__bf16)b[3]; return r; }
DI bf16x4 pack4(f32x4 a) { bf16x4 r; r[0] = (__bf16)a[0]; r[1] = (__bf16)a[1]; r[2] = (__bf16)a[2]; r[3] = (__bf16)a[3]; return r; }
DI bf16x8 neg8(bf16x8 a) { u32x4 u = __builtin_bit_cast(u32x4, a); u ^= 0x80008000u; return __builtin_bit_cast(bf16x8, u); }
DI float sum_g(float v) {
  unsigned u = __builtin_bit_cast(unsigned, v);
  auto a = __builtin_amdgcn_permlane32_swap(u, u, false, false);
  v = __builtin_bit_cast(float, (unsigned)a[0]) + __builtin_bit_cast(float, (unsigned)a[1]);
  u = __builtin_bit_cast(unsigned, v);
  auto b = __builtin_amdgcn_permlane16_swap(u, u, false, false);
  return __builtin_bit_cast(float, (unsigned)b[0]) + __builtin_bit_cast(float, (unsigned)b[1]);
}
DI float bperm(float v, int src_lane) { return __builtin_bit_cast(float, __builtin_amdgcn_ds_bpermute(src_lane * 4, __builtin_bit_cast(int, v))); }
DI u32x4 ld16(rsrc_t r, int voff, int soff) { return __builtin_amdgcn_raw_buffer_load_b128(r, voff, soff, 16); }
DI void st16(u32x4 v, rsrc_t r, int voff, int soff) { __builtin_amdgcn_raw_buffer_store_b128(v, r, voff + soff, 0, 16); }
DI void st_flag(u32x2 ep, rsrc_t r, int off) { __builtin_amdgcn_raw_buffer_store_b64(ep, r, off, 0, 16); }

constexpr int S_ = 2048, D_ = 512, DFF = 1792;
constexpr int WS_E = 0, WS_F1 = 4096, WS_F2 = 8192, WS_F3 = 12288, WS_DBG = 16384;
constexpr int WS_AT = 131072;
constexpr int WS_WT = WS_AT + 256 * 8192;
constexpr int WS_WS = WS_WT + 256 * 8192;
constexpr int WS_H = WS_WS + 32768;
constexpr int WS_BT = WS_H + 128 * 56 * 1024;
constexpr int WS_END = WS_BT + 512 * 4096;
constexpr int UNIT = 8192, RING = 16 * UNIT, L_Y = RING, Y_P = 96, Y_IM = 64 * Y_P, L_V = L_Y + 2 * Y_IM, V_P = 144, V_IM = 32 * V_P, L_CS = L_V + 2 * V_IM, LDS_BYTES = L_CS + 512;
constexpr int U_X = 1, U_W1 = 17, U_FC = 33, U_W2 = 40, U_G = 68, U_END = 76;
constexpr int NB = 25;
constexpr int c_min(int a, int b) { return a < b ? a : b; }
constexpr int c_need(int b) { return b < 4 ? 5 + 4 * b : b < 12 ? 19 + 2 * (b - 4) : b == 12 ? 37 : b < 15 ? 40 : b < 22 ? c_min(44 + 4 * (b - 15), 68) : b < 24 ? 68 : 76; }
constexpr int c_free(int b) { return b <= 0 ? 0 : b < 5 ? 1 + 4 * b : b < 13 ? 19 + 2 * (b - 5) : b == 13 ? 37 : b < 16 ? 40 : b < 22 ? 44 + 4 * (b - 16) : 68; }
#define W_EARLY 6
#define B_EARLY 8
constexpr int U_R0 = 56, U_R1 = 68;
constexpr int c_hi(int b) {
  int hi = c_min(c_free(b) + 16, U_END);
  if (b < B_EARLY) hi = c_min(hi, c_need(b + 1) + W_EARLY);
  return hi;
}
#define B_R0 5
#define R_PER 2
constexpr int c_rhi(int b) { int n = b < B_R0 ? 0 : (b - B_R0 + 1) * R_PER; return n > U_R1 - U_R0 ? U_R1 - U_R0 : n; }
constexpr bool c_isr(int u) { return u >= U_R0 && u < U_R1; }
constexpr int c_after(int b) {
  const int need = c_need(b);
  int u = need - 1;
  while (u >= 0 && c_isr(u)) --u;
  if (u < 0) return 63;
  int bu = -1;
  while (c_hi(bu) <= u) ++bu;
  int n = 0;
  for (int v = u + 1; v < c_hi(b - 1); ++v) if (!c_isr(v)) n += 4;
  n += 4 * (c_rhi(b - 1) - c_rhi(bu - 1));
  return n > 63 ? 63 : n;
}
constexpr int SPIN = 4000;
constexpr int CODE_PF = 27648;
#define B_POST_F1 8
#define B_CONF_F1 11
#define B_POST_F3 20
#define B_CONF_F3 23
#define B_GATH 1
#define B_PF2 6
#define B_FILT 4
#define B_CONF_F2B 17
#define H_PRE 2
constexpr int B_POST_F2A = 12, B_CONF_F2A = 14, B_POST_F2B = 14;

#define TS_DECL
#define TS(i) do { } while (0)
#define TS_OUT(role) do { } while (0)

template <int N> DI void poll_flags(rsrc_t r, int voff, u32x2 ep) {
#pragma unroll 1
  for (int it = 0; it < SPIN; ++it) {
    bool ok;
    if constexpr (N == 0) { u32x2 v = __builtin_amdgcn_raw_buffer_load_b64(r, voff, 0, 16); ok = v[0] == ep[0] && v[1] == ep[1]; }
    else { ok = true;
#pragma unroll
      for (int k = 0; k < N; ++k) { u32x4 v = __builtin_amdgcn_raw_buffer_load_b128(r, voff + 16 * k, 0, 16); ok = ok && v[0] == ep[0] && v[1] == ep[1] && v[2] == ep[0] && v[3] == ep[1]; } }
    if (__builtin_amdgcn_ballot_w64(!ok) == 0) break;
    __builtin_amdgcn_s_sleep(1);
    asm volatile("" ::: "memory");
  }
  asm volatile("" ::: "memory");
}
DI rsrc_t mk_rsrc(const void* p, int bytes) { return __builtin_amdgcn_make_buffer_rsrc((void*)p, 0, bytes, 0x00020000); }
DI unsigned pf(rsrc_t r, int voff) { return __builtin_amdgcn_raw_buffer_load_b32(r, voff, 0, 0); }
DI unsigned ldu16(rsrc_t r, int voff) { return __builtin_amdgcn_raw_buffer_load_b16(r, voff, 0, 0); }
DI void sink(unsigned v) { asm volatile("" ::"v"(v)); }
template <class T> DI T opq(T v) { asm volatile("" : "+v"(v)); return v; }
template <class T> DI void pin(T& v) { asm volatile("" : "+v"(v)); }

DI void dma4(rsrc_t r, lptr dst, int v0, int v1, int v2, int v3, int so) {
  __builtin_amdgcn_raw_ptr_buffer_load_lds(r, (LAS void*)dst, 16, v0, so, 0, 0);
  __builtin_amdgcn_raw_ptr_buffer_load_lds(r, (LAS void*)dst, 16, v1, so, 1024, 0);
  __builtin_amdgcn_raw_ptr_buffer_load_lds(r, (LAS void*)dst, 16, v2, so, 2048, 0);
  __builtin_amdgcn_raw_ptr_buffer_load_lds(r, (LAS void*)dst, 16, v3, so, 3072, 0);
}
constexpr int c_w2row(int ks, int hb) { return 128 * (ks >> 1) + 64 * hb + 32 * (ks & 1); }

DI void dma_role(char* ws, lptr lds, const char* x, const char* w1, const char* w2, int id, int rb, int cg, int cb, int rot, int dw, int lane) {
  TS_DECL;
  TS(62);
  const int e = lane >> 3, cp = lane & 7;
  const int vlin = lane * 16;
  const int vx = (lane >> 4) * 32768 + ((lane & 15) ^ (lane >> 4)) * 16;
  const int vch = (((cp >> 1) ^ ((e >> 1) & 1)) * 32) + (cp & 1) * 16;
  const int vw1 = e * 3584 + vch, vw2 = (16 * (e >> 2) + (e & 3)) * 1024 + vch;
  const int x1 = vx ^ 64, x2 = vx ^ 128, x3 = vx ^ 192;
  const int a1 = (vw1 ^ 64) + 27648, a2 = vw1 + 2 * 27648, a3 = (vw1 ^ 64) + 3 * 27648;
  const int b1 = (vw2 ^ 64) + 3072, b2 = vw2 + 2 * 3072, b3 = (vw2 ^ 64) + 3 * 3072;
  const rsrc_t rX = mk_rsrc(x, S_ * D_ * 2), rW1 = mk_rsrc(w1, D_ * DFF * 2), rW2 = mk_rsrc(w2, D_ * DFF * 2);
  const rsrc_t rF = mk_rsrc(&d_FT, 131072), rC = mk_rsrc(&d_FC, 57344), rG = mk_rsrc(&d_GT, 524288);
  const int cgw = cg < 14 ? cg : 13;
  const int sx = (32 * (32 * (rb & 1) + 16 * dw) + 4 * (rb >> 1)) * 1024;
  const int sw1 = 32 * dw * 3584 + cgw * 256, sw2 = cb * 128;
  const int sf = cg * 8192 + dw * 4096, sc = dw * 4096, sg = cb * 65536 + dw * 4096;
  lptr ld = lds + dw * 4096;
  auto issue = [&](auto U) {
    constexpr int u = U;
    lptr dst = ld + (u & 15) * UNIT;
    if constexpr (u < U_X) dma4(rF, dst, vlin, vlin, vlin, vlin, sf);
    else if constexpr (u < U_W1) { constexpr int st = (u - U_X) >> 2, w = (u - U_X) & 3; dma4(rX, dst, vx, x1, x2, x3, sx + w * 131072 + ((st + rot) & 3) * 256); }
    else if constexpr (u < U_FC) { constexpr int q = u - U_W1; dma4(rW1, dst, vw1, a1, a2, a3, sw1 + (((q & 7) + 2 * rot) & 7) * (64 * 3584) + (q >> 3) * 128); }
    else if constexpr (u < U_W2) dma4(rC, dst, vlin, vlin, vlin, vlin, sc + (u - U_FC) * 8192);
    else if constexpr (u < U_G) {
      constexpr int q = u - U_W2, hb = q / 14, i = (q % 14) >> 1, p = q & 1;
      dma4(rW2, dst, vw2, b1, b2, b3, sw2 + (dw ? c_w2row(7 * (2 * p + 1) + i, hb) : c_w2row(7 * (2 * p) + i, hb)) * 1024);
    }
    else dma4(rG, dst, vlin, vlin, vlin, vlin, sg + (u - U_G) * 8192);
  };
  u32x4 rg[U_R1 - U_R0][4];
  auto rload = [&](auto U) {
    constexpr int u = U, q = u - U_W2, hb = q / 14, i = (q % 14) >> 1, p = q & 1;
    const int so = sw2 + (dw ? c_w2row(7 * (2 * p + 1) + i, hb) : c_w2row(7 * (2 * p) + i, hb)) * 1024;
    rg[u - U_R0][0] = __builtin_amdgcn_raw_buffer_load_b128(rW2, vw2, so, 0);
    rg[u - U_R0][1] = __builtin_amdgcn_raw_buffer_load_b128(rW2, b1 + 1024, so, 0);
    rg[u - U_R0][2] = __builtin_amdgcn_raw_buffer_load_b128(rW2, b2 + 2048, so, 0);
    rg[u - U_R0][3] = __builtin_amdgcn_raw_buffer_load_b128(rW2, b3 + 3072, so, 0);
  };
  auto place = [&](auto U) {
    constexpr int u = U;
    if constexpr (c_isr(u)) {
#pragma unroll
      for (int i = 0; i < 4; ++i) *(LAS u32x4*)(ld + (u & 15) * UNIT + i * 1024 + lane * 16) = rg[u - U_R0][i];
    } else issue(U);
  };
  sfor<c_hi(-1)>([&](auto U) { issue(U); });
  TS(63);
  sfor<NB>([&](auto B) {
    constexpr int b = B;
    constexpr int issued = c_hi(b - 1);
    static_assert(issued >= c_need(b), "schedule");
    wait_vm<c_after(b)>();
    wait_lgkm0();
    TS(b);
    bar();
    constexpr int hi = c_hi(b);
    static_assert(hi >= issued, "schedule");
    sfor<hi - issued>([&](auto I) { place(std::integral_constant<int, issued + I>{}); });
    sbar();
    sfor<c_rhi(b) - c_rhi(b - 1)>([&](auto I) { rload(std::integral_constant<int, U_R0 + c_rhi(b - 1) + I>{}); });
    sbar();
    if constexpr (b == 13) { if (id == 0) { bar(); bar(); bar(); } }
  });
  if (dw == 0) TS_OUT(1);
}

typedef unsigned u32a2 __attribute__((aligned(2)));
DI void sync_role(lptr lds, const char* __restrict__ wr, const char* __restrict__ wi, const char* w1, const char* w2, char* ws, const char* code, int id, int rb, int cg, int n2t, int lane) {
  TS_DECL;
  TS(62);
  const rsrc_t rs = mk_rsrc(ws, WS_END);
  const int xcd = id & 7, loc = id >> 3;
  const u32x2 e0 = __builtin_amdgcn_raw_buffer_load_b64(rs, WS_E, 0, 16);
  unsigned p0[4], p1[9];
  { const rsrc_t rI = mk_rsrc(code, CODE_PF), rC = mk_rsrc(&d_FC, 57344);
    p0[0] = pf(rI, ((loc & 3) * 64 + lane) * 128);
    p0[3] = pf(rC, lane < 14 ? (loc * 14 + lane) * 128 : 0x7ffffff0); }
  u64 E = ((u64)(unsigned)__builtin_amdgcn_readfirstlane(e0[1]) << 32) | (unsigned)__builtin_amdgcn_readfirstlane(e0[0]);
  E = E * 6364136223846793005ull + 1442695040888963407ull;
  const u32x2 ep = {(unsigned)E, (unsigned)(E >> 32)};
  TS(63);
  const int wk = rb >> 2, gk = rb & 3;
  unsigned fa[4][4], fb[4][4], sp[2][8];
  sfor<NB>([&](auto B) {
    constexpr int b = B;
    if constexpr (b == B_CONF_F1) poll_flags<0>(rs, WS_F1 + ((id >> 4) * 16 + (lane & 15)) * 8, ep);
    if constexpr (b == B_CONF_F2A) poll_flags<0>(rs, WS_F2 + ((2 * (n2t >> 2)) * 16 + (lane & 31)) * 8, ep);
    if constexpr (b == B_CONF_F2B) poll_flags<0>(rs, WS_F2 + 2048 + ((2 * (n2t >> 2)) * 16 + (lane & 31)) * 8, ep);
    if constexpr (b == B_CONF_F3) poll_flags<2>(rs, WS_F3 + lane * 32, ep);
    if constexpr (b == B_POST_F1) wait_vm<(B_PF2 > B_FILT + 1 && B_PF2 < B_POST_F1 ? 9 : 0)>();
    if constexpr (b == B_POST_F3) wait_vm<0>();
    TS(b);
    bar();
    TS(31 + b);
    if constexpr (b == B_POST_F1) { if (lane == 0) st_flag(ep, rs, WS_F1 + (cg * 16 + rb) * 8); }
    if constexpr (b == B_POST_F2A) { if (lane == 0) st_flag(ep, rs, WS_F2 + (rb * 16 + cg) * 8); }
    if constexpr (b == B_POST_F2B) { if (lane == 0) st_flag(ep, rs, WS_F2 + 2048 + (rb * 16 + cg) * 8); }
    if constexpr (b == B_POST_F3) { if (lane == 0) st_flag(ep, rs, WS_F3 + id * 8); }
    if constexpr (b == B_CONF_F3) { if (id == 0 && lane == 0) st_flag(ep, rs, WS_E); }
    if constexpr (b == 0) {
      const rsrc_t rW1 = mk_rsrc(w1, D_ * DFF * 2);
      p0[1] = pf(rW1, (16 * loc + (lane >> 3)) * 3584 + (xcd >> 1) * 1024 + (lane & 7) * 128);
      p0[2] = pf(rW1, (16 * loc + 8 + (lane >> 3)) * 3584 + (xcd >> 1) * 1024 + (lane & 7) * 128);
    }
    if constexpr (b == B_GATH || b == B_GATH + 1) {
#pragma unroll
      for (int R = 2 * (b - B_GATH); R < 2 * (b - B_GATH) + 2; ++R)
#pragma unroll
        for (int r = 0; r < 4; ++r) {
          const int k = 16 * wk + 8 * (R >> 1) + (lane >> 3) + 64 * (16 * (R & 1) + 4 * gk + r), o = k * 514 + (16 * cg + 2 * (lane & 7)) * 2;
          fa[R][r] = *(const u32a2*)(wr + o); fb[R][r] = *(const u32a2*)(wi + o);
        }
      sbar();
    }
    if constexpr (b == B_GATH + 2) {
      const rsrc_t rR = mk_rsrc(wr, cg == 0 ? S_ * 257 * 2 : 0), rI = mk_rsrc(wi, cg == 0 ? S_ * 257 * 2 : 0);
#pragma unroll
      for (int it = 0; it < 2; ++it) {
        const int e = 4 * it + (lane >> 4), l = lane & 15;
        const int k = 16 * wk + l + 64 * (16 * (e >> 2) + 4 * gk + (e & 3)), km = (2048 - k) & 2047;
        sp[it][0] = ldu16(rR, k * 514); sp[it][1] = ldu16(rI, k * 514); sp[it][2] = ldu16(rR, km * 514); sp[it][3] = ldu16(rI, km * 514);
        sp[it][4] = ldu16(rR, k * 514 + 512); sp[it][5] = ldu16(rI, k * 514 + 512); sp[it][6] = ldu16(rR, km * 514 + 512); sp[it][7] = ldu16(rI, km * 514 + 512);
      }
      sbar();
    }
    if constexpr (b == B_PF2) {
      const rsrc_t rW2 = mk_rsrc(w2, D_ * DFF * 2), rG = mk_rsrc(&d_GT, 524288);
#pragma unroll
      for (int i = 0; i < 7; ++i) p1[i] = pf(rW2, loc * 57344 + (i * 64 + lane) * 128);
#pragma unroll
      for (int i = 0; i < 2; ++i) p1[7 + i] = pf(rG, loc * 16384 + (i * 64 + lane) * 128);
    }
    if constexpr (b == B_FILT) {
      sink(p0[0]); sink(p0[1]); sink(p0[2]); sink(p0[3]);
#pragma unroll
      for (int R = 0; R < 4; ++R) {
        u32x4 v0, v1;
#pragma unroll
        for (int r = 0; r < 4; ++r) { v0[r] = (fa[R][r] & 0xffffu) | (fb[R][r] << 16); v1[r] = (fa[R][r] >> 16) | (fb[R][r] & 0xffff0000u); }
        const int wo = WS_WT + (16 * cg + 2 * (lane & 7)) * 8192 + wk * 2048 + (R & 1) * 1024 + (16 * gk + 8 * (R >> 1) + (lane >> 3)) * 16;
        st16(v0, rs, wo, 0); st16(v1, rs, wo + 8192, 0);
      }
    }
    if constexpr (b == B_FILT + 1) {
#pragma unroll
      for (int it = 0; it < 2; ++it) {
        const int e = 4 * it + (lane >> 4), l = lane & 15;
        const float w0r = bflo(sp[it][0]), w0i = bflo(sp[it][1]), m0r = bflo(sp[it][2]), m0i = bflo(sp[it][3]);
        const float wnr = bflo(sp[it][4]), wni = bflo(sp[it][5]), mnr = bflo(sp[it][6]), mni = bflo(sp[it][7]);
        const float ar = 0.5f * (w0r + m0r), ai = 0.5f * (w0i - m0i), br = 0.5f * (wnr + mnr), bi = 0.5f * (wni - mni);
        const f32x4 uv = {0.25f * (ar + br), 0.25f * (ai + bi), 0.25f * (ar - br), 0.25f * (ai - bi)};
        st16(__builtin_bit_cast(u32x4, uv), rs, cg == 0 ? WS_WS + ((wk * 8 + e) * 64 + 16 * gk + l) * 16 : 0x7f000000, 0);
      }
    }
    if constexpr (b == 9) {
#pragma unroll
      for (int i = 0; i < 9; ++i) sink(p1[i]);
    }
    if constexpr (b == 13) { if (id == 0) { bar(); bar(); bar(); } }
    if constexpr (b == 15) {
      const int cre = 32 * (id >> 4) + (id & 15);
      const int bo = WS_BT + cre * 4096 + (lane >> 3) * 128 + (((lane & 7) ^ (lane >> 3)) * 16);
      u32x4 v[8];
#pragma unroll
      for (int i = 0; i < 8; ++i) v[i] = *(LAS u32x4*)(lds + L_Y + i * 1024 + lane * 16);
#pragma unroll
      for (int i = 0; i < 8; ++i) st16(v[i], rs, bo + (i >> 2) * 65536 + (i & 3) * 1024, 0);
      wait_lgkm0();
    }
  });
  TS_OUT(2);
}
DI void compute_role(lptr lds, const uint16_t* __restrict__ x, char* ws, uint16_t* __restrict__ out, int id, int rb, int cg, int n2t, int cb, int rot, int w, int lane) {
  int g = lane >> 4, li = lane & 15;
  auto fresh = [&]() { lane = opq(lane); g = lane >> 4; li = lane & 15; };
  TS_DECL;
  TS(62);
  [[maybe_unused]] int bi_ = 0;
  auto tbar = [&]() { TS(bi_); bar(); TS(31 + bi_); ++bi_; };
  const rsrc_t rs = mk_rsrc(ws, WS_END);
  const f32x4 z4 = {0.f, 0.f, 0.f, 0.f};
  const bf16x8 ones = __builtin_bit_cast(bf16x8, u32x4{0x3f803f80u, 0x3f803f80u, 0x3f803f80u, 0x3f803f80u});
  bf16x8 xr[2][16];
  f32x4 acc[2][4];
  float mu[2], rsd[2];
  bf16x8 wf[2][4];
  int ta;
  auto rd2 = [&](int buf, int base) {
#pragma unroll
    for (int t = 0; t < 4; ++t) wf[buf][t] = lds_tr2(lds + base + ((ta ^ (w << 5)) ^ (t << 5)));
  };
  {
    f32x4 CS[2][2][2], SU[2] = {z4, z4}, GR[2] = {z4, z4};
    const int xa = li * 256 + ((g ^ li) * 16);
    const unsigned sgn = (unsigned)(li & 1) << 31;
    bf16x8 ff[2][4];
    sfor<4>([&](auto ST) {
      constexpr int st = ST;
      tbar();
      constexpr int xs0 = (U_X + 4 * st) * UNIT;
      const int xs = (xs0 + w * UNIT) & (RING - 1);
      if constexpr (st == 0) {
#pragma unroll
        for (int k = 0; k < 4; ++k) { ff[0][k] = lds_b128(lds + lane * 16 + k * 1024); ff[1][k] = lds_b128(lds + lane * 16 + 4096 + k * 1024); }
      }
#pragma unroll
      for (int k = 0; k < 4; ++k) { xr[0][4 * st + k] = lds_b128(lds + xs + (xa ^ (k << 6))); xr[1][4 * st + k] = lds_b128(lds + xs + (xa ^ (k << 6)) + 4096); }
#pragma unroll
      for (int m = 0; m < 2; ++m)
#pragma unroll
        for (int p = 0; p < 2; ++p) {
          if constexpr (st < 2) CS[st & 1][m][p] = z4;
          else CS[st & 1][m][p] = __builtin_bit_cast(f32x4, __builtin_bit_cast(u32x4, CS[st & 1][m][p]) ^ sgn);
        }
#pragma unroll
      for (int k = 0; k < 4; ++k)
#pragma unroll
        for (int m = 0; m < 2; ++m) {
          CS[st & 1][m][0] = mfma(xr[m][4 * st + k], ff[0][k], CS[st & 1][m][0]); CS[st & 1][m][1] = mfma(xr[m][4 * st + k], ff[1][k], CS[st & 1][m][1]);
          SU[m] = mfma(ones, xr[m][4 * st + k], SU[m]); GR[m] = mfma(xr[m][4 * st + k], xr[m][4 * st + k], GR[m]);
        }
#pragma unroll
      for (int m = 0; m < 2; ++m) { pin(CS[st & 1][m][0]); pin(CS[st & 1][m][1]); pin(SU[m]); pin(GR[m]); }
      wait_lgkm0();
    });
    fresh();
    ta = (8 * g + (li >> 2)) * 128 + (li & 3) * 8 + ((((li >> 3) & 1) + 2 * (g & 1)) * 32);
    tbar();
    rd2(0, (U_W1 & 15) * UNIT);
    sbar();
#pragma unroll
    for (int m = 0; m < 2; ++m) {
      float dg = (li & 2) ? ((li & 1) ? GR[m][3] : GR[m][2]) : ((li & 1) ? GR[m][1] : GR[m][0]);
      dg = g == (li >> 2) ? dg : 0.f;
      const float s2 = sum_g(dg);
      const float mean = SU[m][0] * (1.f / 512.f);
      const float var = fmaxf(s2 * (1.f / 512.f) - mean * mean, 0.f);
      const float den = bf_round(__builtin_amdgcn_sqrtf(bf_round(bf_round(var) + 1e-5f)));
      mu[m] = bf_round(mean); rsd[m] = __builtin_amdgcn_rcpf(den);
    }
    float sr[2], si[2]; bool sw[2];
#pragma unroll
    for (int p = 0; p < 2; ++p) { const int q = ((li & 3) * ((2 + p + rot) & 3)) & 3; sw[p] = q & 1; sr[p] = q < 2 ? 1.f : -1.f; si[p] = (q == 0 || q == 3) ? 1.f : -1.f; }
    const float csf = (cg == 0 && li == 0) ? 512.f : 0.f;
    f32x4 v[2][2];
#pragma unroll
    for (int m = 0; m < 2; ++m)
#pragma unroll
      for (int r = 0; r < 4; ++r) {
        const float r4 = bperm(rsd[m], 4 * g + r), m4 = bperm(mu[m], 4 * g + r);
        const float re = sr[0] * (sw[0] ? CS[0][m][1][r] : CS[0][m][0][r]) + sr[1] * (sw[1] ? CS[1][m][1][r] : CS[1][m][0][r]);
        const float im = si[0] * (sw[0] ? CS[0][m][0][r] : CS[0][m][1][r]) + si[1] * (sw[1] ? CS[1][m][0][r] : CS[1][m][1][r]);
        v[m][0][r] = (re - m4 * csf) * r4; v[m][1][r] = im * r4;
      }
    const int ao = WS_AT + (16 * cg + li) * 8192 + (rb >> 1) * 512 + (rb & 1) * 256 + w * 64 + g * 16;
    st16(__builtin_bit_cast(u32x4, pack8(v[0][0], v[1][0])), rs, ao, 0);
    st16(__builtin_bit_cast(u32x4, pack8(v[0][1], v[1][1])), rs, ao + 4096, 0);
    sbar();
  }
  const int j = id, pu = w & 1, pv = w >> 1;
  auto h_epilogue = [&](int hb) {
    fresh();
    const float mu_[2] = {opq(mu[0]), opq(mu[1])}, rsd_[2] = {opq(rsd[0]), opq(rsd[1])};
    const bool odd = w & 1;
    const int hoff = cg < 14 ? WS_H + (((4 * (rb >> 1) + (li >> 2)) * 4 + 2 * (rb & 1)) * 56) * 1024 + (16 * g + 4 * w + (li & 3)) * 16 : 0x7f000000;
#pragma unroll
    for (int m = 0; m < 2; ++m)
#pragma unroll
      for (int u = 0; u < 2; ++u) {
        f32x4 v[2];
#pragma unroll
        for (int t = 0; t < 2; ++t) {
          const f32x4 c4 = lds_f4(lds + L_CS + hb * 256 + (((2 * u + t) ^ w) * 64) + g * 16);
          v[t] = (acc[m][2 * u + t] - c4 * mu_[m]) * rsd_[m];
#pragma unroll
          for (int e = 0; e < 4; ++e) v[t][e] = fmaxf(v[t][e], 0.f);
        }
        const u32x2 p0 = __builtin_bit_cast(u32x2, pack4(v[0])), p1 = __builtin_bit_cast(u32x2, pack4(v[1]));
        const u32x4 o = {odd ? p1[0] : p0[0], odd ? p1[1] : p0[1], odd ? p0[0] : p1[0], odd ? p0[1] : p1[1]};
        const int ks56 = hb * 28 + 2 * cg + (u ^ (w >> 1));
        st16(o, rs, hoff + m * 56 * 1024, ks56 * 1024);
      }
  };
  bf16x8 zr[2], zi[2];
  u32x4 wq[2];
  sfor<2>([&](auto HB) {
    constexpr int hb = HB;
#pragma unroll
    for (int m = 0; m < 2; ++m)
#pragma unroll
      for (int t = 0; t < 4; ++t) acc[m][t] = z4;
    f32x4 csa = z4;
    sfor<16>([&](auto KS) {
      constexpr int ks = KS, cur = ks & 1, nxt = cur ^ 1;
      sbar();
      if constexpr (ks < 15) {
        if constexpr ((ks & 3) == 3) {
          constexpr int b = 5 + 4 * hb + (ks >> 2);
          wait_lgkm0();
          if constexpr (b == B_POST_F1) wait_vm<(b > 8 ? 4 : 0)>();
          tbar();
          if constexpr (b == B_CONF_F1) {
            fresh();
            const int zo = WS_AT + j * 8192 + (4 * pu + (li >> 2)) * 512 + g * 64 + (li & 3) * 16;
#pragma unroll
            for (int k = 0; k < 2; ++k) { zr[k] = __builtin_bit_cast(bf16x8, ld16(rs, zo + k * 256, 0)); zi[k] = __builtin_bit_cast(bf16x8, ld16(rs, zo + 4096 + k * 256, 0)); }
            wq[0] = ld16(rs, WS_WT + j * 8192 + w * 2048 + lane * 16, 0); wq[1] = ld16(rs, WS_WT + j * 8192 + w * 2048 + 1024 + lane * 16, 0);
            sbar();
          }
        }
        constexpr int un = U_W1 + 8 * hb + ((ks + 1) >> 1);
        rd2(nxt, (un & 15) * UNIT + ((ks + 1) & 1) * 4096);
      }
#pragma unroll
      for (int t = 0; t < 4; ++t)
#pragma unroll
        for (int m = 0; m < 2; ++m) acc[m][t] = mfma(wf[cur][t], xr[m][ks], acc[m][t]);
      csa = mfma(wf[cur][0], ones, csa);
      if constexpr (ks < 15) {
#pragma unroll
        for (int i = 0; i < 8; ++i) { __builtin_amdgcn_sched_group_barrier(0x008, 1, 0); __builtin_amdgcn_sched_group_barrier(0x100, 1, 0); }
        __builtin_amdgcn_sched_group_barrier(0x008, 1, 0);
      }
    });
    sbar();
    if (li == 0) *(LAS f32x4*)(lds + L_CS + hb * 256 + w * 64 + g * 16) = csa;
    wait_lgkm0();
    if constexpr (hb == 0 && B_POST_F1 == 8) wait_vm<0>();
    if constexpr (hb == 1) wait_vm<(B_CONF_F1 <= 11 ? 6 : 0)>();
    tbar();
    if constexpr (hb == 0) rd2(0, ((U_W1 + 8) & 15) * UNIT);
    if constexpr (hb == 1 && B_CONF_F1 == 12) {
      fresh();
      const int zo = WS_AT + j * 8192 + (4 * pu + (li >> 2)) * 512 + g * 64 + (li & 3) * 16;
#pragma unroll
      for (int k = 0; k < 2; ++k) { zr[k] = __builtin_bit_cast(bf16x8, ld16(rs, zo + k * 256, 0)); zi[k] = __builtin_bit_cast(bf16x8, ld16(rs, zo + 4096 + k * 256, 0)); }
      wq[0] = ld16(rs, WS_WT + j * 8192 + w * 2048 + lane * 16, 0); wq[1] = ld16(rs, WS_WT + j * 8192 + w * 2048 + 1024 + lane * 16, 0);
    }
    sbar();
    h_epilogue(hb);
    sbar();
  });
  bf16x8 hr[4][7];
  fresh();
  const int ho = WS_H + (n2t * 4 * 56 + 7 * w) * 1024 + lane * 16;
  auto ld_h = [&](int i, int half) {
#pragma unroll
    for (int rt = 0; rt < 4; ++rt) hr[rt][i] = __builtin_bit_cast(bf16x8, ld16(rs, ho + i * 1024, ((rt ^ (w & 2)) * 56 + 28 * half) * 1024));
  };
  {
    lptr fc = lds + (U_FC & 15) * UNIT + lane * 16;
    bf16x8 c64[2][2][2];
    {
      f32x4 tw1[2][2];
#pragma unroll
      for (int t = 0; t < 2; ++t)
#pragma unroll
        for (int p = 0; p < 2; ++p) {
#pragma unroll
          for (int ks = 0; ks < 2; ++ks) c64[t][ks][p] = lds_b128(fc + pv * 8192 + ((t * 2 + ks) * 2 + p) * 1024);
          tw1[t][p] = lds_f4(fc + 16384 + w * 4096 + (t * 2 + p) * 1024);
        }
#pragma unroll
      for (int t = 0; t < 2; ++t) {
        f32x4 yr = z4, yi = z4;
#pragma unroll
        for (int ks = 0; ks < 2; ++ks) {
          yr = mfma(zr[ks], c64[t][ks][0], yr); yr = mfma(neg8(zi[ks]), c64[t][ks][1], yr);
          yi = mfma(zi[ks], c64[t][ks][0], yi); yi = mfma(zr[ks], c64[t][ks][1], yi);
        }
        const f32x4 pr = yr * tw1[t][0] + yi * tw1[t][1], pi = yi * tw1[t][0] - yr * tw1[t][1];
        lptr p = lds + L_Y + (32 * pv + 8 * (li >> 2) + 4 * t + (li & 3)) * Y_P + (8 * g + 4 * pu) * 2;
        *(LAS bf16x4*)p = pack4(pr); *(LAS bf16x4*)(p + Y_IM) = pack4(pi);
      }
    }
    wait_lgkm0();
    tbar();
    bf16x8 c32[2][2];
    f32x4 tw3[2][2];
#pragma unroll
    for (int t = 0; t < 2; ++t)
#pragma unroll
      for (int p = 0; p < 2; ++p) { c32[t][p] = lds_b128(fc + 32768 + (t * 2 + p) * 1024); tw3[t][p] = lds_f4(fc + 40960 + w * 4096 + (t * 2 + p) * 1024); }
    f32x4 xre[2], xim[2];
    {
      lptr p = lds + L_Y + (16 * w + li) * Y_P + g * 16;
      const bf16x8 ur = lds_b128(p), ui = lds_b128(p + Y_IM), nui = neg8(ui);
#pragma unroll
      for (int t = 0; t < 2; ++t) {
        xre[t] = mfma(c32[t][0], ur, z4); xre[t] = mfma(c32[t][1], nui, xre[t]);
        xim[t] = mfma(c32[t][1], ur, z4); xim[t] = mfma(c32[t][0], ui, xim[t]);
      }
    }
    f32x4 pr[2], pi[2];
    if (j != 0) {
#pragma unroll
      for (int t = 0; t < 2; ++t)
#pragma unroll
        for (int r = 0; r < 4; ++r) {
          const float wre = bflo(wq[t][r]), wim = bfhi(wq[t][r]);
          pr[t][r] = xre[t][r] * wre - xim[t][r] * wim; pi[t][r] = xre[t][r] * wim + xim[t][r] * wre;
        }
    } else {
      wait_lgkm0();
      bar();
      LAS f32x2* X = (LAS f32x2*)(lds + L_Y);
#pragma unroll
      for (int t = 0; t < 2; ++t)
#pragma unroll
        for (int r = 0; r < 4; ++r) X[16 * w + li + 64 * (16 * t + 4 * g + r)] = f32x2{xre[t][r], xim[t][r]};
      wait_lgkm0();
      bar();
#pragma unroll
      for (int t = 0; t < 2; ++t)
#pragma unroll
        for (int r = 0; r < 4; ++r) {
          const int k = 16 * w + li + 64 * (16 * t + 4 * g + r);
          const f32x2 xm = X[(2048 - k) & 2047];
          const f32x4 c = __builtin_bit_cast(f32x4, ld16(rs, WS_WS + ((w * 8 + 4 * t + r) * 64 + lane) * 16, 0));
          pr[t][r] = xre[t][r] * c[0] - xim[t][r] * c[1] + xm[0] * c[2] + xm[1] * c[3];
          pi[t][r] = xre[t][r] * c[1] + xim[t][r] * c[0] + xm[0] * c[3] - xm[1] * c[2];
        }
      wait_lgkm0();
      bar();
    }
    {
      const bf16x8 Pr = pack8(pr[0], pr[1]), Pi = pack8(pi[0], pi[1]), nPr = neg8(Pr);
#pragma unroll
      for (int t = 0; t < 2; ++t) {
        f32x4 vr = mfma(Pr, c32[t][0], z4); vr = mfma(Pi, c32[t][1], vr);
        f32x4 vi = mfma(Pi, c32[t][0], z4); vi = mfma(nPr, c32[t][1], vi);
        const f32x4 ar = vr * tw3[t][0] - vi * tw3[t][1], ai = vr * tw3[t][1] + vi * tw3[t][0];
        lptr p = lds + L_V + (16 * t + li) * V_P + (32 * (w >> 1) + 8 * g + 4 * (w & 1)) * 2;
        *(LAS bf16x4*)p = pack4(ar); *(LAS bf16x4*)(p + V_IM) = pack4(ai);
      }
    }
    wait_lgkm0();
    wait_vm<0>();
    tbar();
    sbar();
#pragma unroll
    for (int i = 0; i < H_PRE; ++i) ld_h(i, 0);
    sbar();
    {
      bf16x8 vr[2], vi[2];
#pragma unroll
      for (int ks = 0; ks < 2; ++ks) { lptr p = lds + L_V + (16 * pu + li) * V_P + (32 * ks + 8 * g) * 2; vr[ks] = lds_b128(p); vi[ks] = lds_b128(p + V_IM); }
      f32x4 yr[2], yi[2];
#pragma unroll
      for (int t = 0; t < 2; ++t) {
        yr[t] = z4; yi[t] = z4;
#pragma unroll
        for (int ks = 0; ks < 2; ++ks) {
          yr[t] = mfma(c64[t][ks][0], vr[ks], yr[t]); yr[t] = mfma(c64[t][ks][1], vi[ks], yr[t]);
          yi[t] = mfma(c64[t][ks][0], vi[ks], yi[t]); yi[t] = mfma(c64[t][ks][1], neg8(vr[ks]), yi[t]);
        }
      }
      lptr p = lds + L_Y + (16 * pu + li) * 128 + (((4 * pv + g) ^ (li & 7)) * 16);
      *(LAS bf16x8*)p = pack8(yr[0], yr[1]); *(LAS bf16x8*)(p + 4096) = pack8(yi[0], yi[1]);
    }
    sbar();
  }
  f32x4 acc1[4][4];
  {
    fresh();
    ta = (8 * g + (li >> 2)) * 128 + (li & 3) * 8 + ((((li >> 3) & 1) + 2 * (g & 1)) * 32);
    const int tw = (ta ^ ((w & 1) << 6)) + w * 4096;
    wait_lgkm0();
    tbar();
    bf16x8 wg[2][4];
    auto rd4 = [&](int buf, int base) {
#pragma unroll
      for (int t = 0; t < 4; ++t) wg[buf][t] = lds_tr2(lds + base + (tw ^ (t << 5)));
    };
    rd4(0, (U_W2 & 15) * UNIT);
#pragma unroll
    for (int rt = 0; rt < 4; ++rt)
#pragma unroll
      for (int t = 0; t < 4; ++t) acc1[rt][t] = z4;
    sfor<14>([&](auto SG) {
      constexpr int sg = SG, cur = sg & 1, nxt = cur ^ 1, i = sg % 7;
      sbar();
      constexpr int sgc = 2 * (B_CONF_F2B - 16) + 1;
      if constexpr ((sg & 1) && sg < 13) {
        wait_lgkm0();
        tbar();
        if constexpr (sg == sgc) {
#pragma unroll
          for (int q = 0; q < sgc; ++q) ld_h(q, 1);
          sbar();
        }
      }
      if constexpr (sg == 0) { ld_h(H_PRE, 0); if constexpr (H_PRE + 1 < 7) ld_h(H_PRE + 1, 0); if constexpr (H_PRE + 2 < 7) ld_h(H_PRE + 2, 0); sbar(); }
      if constexpr (sg == 1) {
#pragma unroll
        for (int q = H_PRE + 3; q < 7; ++q) ld_h(q, 0);
        sbar();
      }
      if constexpr (sg < 13) rd4(nxt, ((U_W2 + 2 * (sg + 1)) & 15) * UNIT);
#pragma unroll
      for (int t = 0; t < 4; ++t)
#pragma unroll
        for (int rt = 0; rt < 4; ++rt) acc1[rt][t] = mfma(wg[cur][t], hr[rt][i], acc1[rt][t]);
      if constexpr (sg < 13) {
#pragma unroll
        for (int q = 0; q < 8; ++q) { __builtin_amdgcn_sched_group_barrier(0x008, 2, 0); __builtin_amdgcn_sched_group_barrier(0x100, 1, 0); }
      }
      if constexpr (sg >= sgc && sg < 7) { sbar(); ld_h(i, 1); }
    });
    sbar();
  }
  fresh();
  ta = (8 * g + (li >> 2)) * 128 + (li & 3) * 8 + ((((li >> 3) & 1) + 2 * (g & 1)) * 32);
  u32x2 xv[2][2];
  f32x4 gate[2][2], acc2[2][2];
  const int col0 = 64 * cb + 32 * (w & 1);
  {
    auto red = [&](int o, int d) { const int grp = o * 3 + d - 1; return lds + (grp < 8 ? 12 * UNIT + grp * 4096 : L_Y + (grp - 8) * 4096) + lane * 16; };
#pragma unroll
    for (int d = 1; d < 4; ++d) {
      lptr p = red(w ^ d, d);
#pragma unroll
      for (int t = 0; t < 4; ++t) *(LAS f32x4*)(p + t * 1024) = acc1[2 * (d >> 1) + (t >> 1)][2 * (d & 1) + (t & 1)];
    }
#pragma unroll
    for (int a = 0; a < 2; ++a)
#pragma unroll
      for (int c = 0; c < 2; ++c) xv[a][c] = *(const u32x2*)(x + (size_t)(32 * (16 * ((w & 2) + a) + li) + n2t) * 512 + col0 + 16 * c + 4 * g);
    wait_lgkm0();
    tbar();
    u32x4 bq[16];
    const int e8 = lane >> 3, cp = lane & 7;
    const int bo = WS_BT + (16 * w + e8) * 4096 + n2t * 128 + cp * 16;
    auto ld_b = [&]() {
#pragma unroll
      for (int b = 0; b < 8; ++b) { bq[2 * b] = ld16(rs, bo, b * 64 * 4096); bq[2 * b + 1] = ld16(rs, bo, b * 64 * 4096 + 8 * 4096); }
      sbar();
    };
    if constexpr (B_CONF_F3 == 22) ld_b();
#pragma unroll
    for (int d = 1; d < 4; ++d) {
      lptr p = red(w, d);
#pragma unroll
      for (int t = 0; t < 4; ++t) acc1[t >> 1][t & 1] += lds_f4(p + t * 1024);
    }
#pragma unroll
    for (int a = 0; a < 2; ++a)
#pragma unroll
      for (int c = 0; c < 2; ++c)
#pragma unroll
        for (int r = 0; r < 4; ++r) { const float v = bf_round(acc1[a][c][r]); gate[a][c][r] = bf_round(v * __builtin_amdgcn_rcpf(1.f + __expf(-v))); }
#pragma unroll
    for (int a = 0; a < 2; ++a) { pin(gate[a][0]); pin(gate[a][1]); }
    wait_lgkm0();
    tbar();
    if constexpr (B_CONF_F3 == 23) ld_b();
    const int lo = (16 * w + e8) * 128 + (((cp >> 1) ^ ((e8 >> 1) & 1)) * 32) + (cp & 1) * 16;
#pragma unroll
    for (int b = 0; b < 8; ++b) {
      lptr p = lds + ((U_END + b) & 15) * UNIT;
      *(LAS u32x4*)(p + lo) = bq[2 * b]; *(LAS u32x4*)(p + ((lo + 1024) ^ 64)) = bq[2 * b + 1];
    }
    wait_lgkm0();
    tbar();
    bf16x8 gf[2][4], bf[2][4];
    const int go = lane * 16 + (w & 1) * 4096, bs = ta ^ ((w & 2) << 5);
    auto rd5 = [&](int buf, int b) {
#pragma unroll
      for (int k = 0; k < 2; ++k)
#pragma unroll
        for (int c = 0; c < 2; ++c) {
          gf[buf][k * 2 + c] = lds_b128(lds + ((U_G + b) & 15) * UNIT + go + (c * 2 + k) * 1024);
          bf[buf][k * 2 + c] = lds_tr2(lds + ((U_END + b) & 15) * UNIT + (bs ^ (c << 5)) + k * 4096);
        }
    };
    rd5(0, 0);
#pragma unroll
    for (int a = 0; a < 2; ++a) { acc2[a][0] = z4; acc2[a][1] = z4; }
    sfor<8>([&](auto B) {
      constexpr int b = B, cur = b & 1;
      sbar();
      if constexpr (b < 7) rd5(cur ^ 1, b + 1);
#pragma unroll
      for (int k = 0; k < 2; ++k)
#pragma unroll
        for (int a = 0; a < 2; ++a)
#pragma unroll
          for (int c = 0; c < 2; ++c) acc2[a][c] = mfma(gf[cur][k * 2 + c], bf[cur][k * 2 + a], acc2[a][c]);
      if constexpr (b < 7) {
#pragma unroll
        for (int q = 0; q < 4; ++q) { __builtin_amdgcn_sched_group_barrier(0x008, 2, 0); __builtin_amdgcn_sched_group_barrier(0x100, 3, 0); }
      }
    });
    sbar();
  }
#pragma unroll
  for (int a = 0; a < 2; ++a) {
    u32x2 o[2];
#pragma unroll
    for (int c = 0; c < 2; ++c) {
      f32x4 r4;
#pragma unroll
      for (int r = 0; r < 4; ++r) {
        const float sv = bf_round(acc2[a][c][r] * (1.f / 524288.f));
        const float xin = (r & 1) ? bfhi(xv[a][c][r >> 1]) : bflo(xv[a][c][r >> 1]);
        r4[r] = xin + bf_round(sv * gate[a][c][r]);
      }
      o[c] = __builtin_bit_cast(u32x2, pack4(r4));
    }
    auto s0 = __builtin_amdgcn_permlane16_swap(o[0][0], o[1][0], false, false);
    auto s1 = __builtin_amdgcn_permlane16_swap(o[0][1], o[1][1], false, false);
    const u32x4 q = {(unsigned)s0[0], (unsigned)s1[0], (unsigned)s0[1], (unsigned)s1[1]};
    *(u32x4*)(out + (size_t)(32 * (16 * ((w & 2) + a) + li) + n2t) * 512 + col0 + 16 * (g & 1) + 8 * (g >> 1)) = q;
  }
  TS(63);
  if (w == 0) TS_OUT(0);
}

__global__ __launch_bounds__(448, 2) void fused_k(const uint16_t* __restrict__ x, const uint16_t* __restrict__ wr, const uint16_t* __restrict__ wi,
                                                   const uint16_t* __restrict__ w1, const uint16_t* __restrict__ w2, char* __restrict__ ws, uint16_t* __restrict__ out) {
  extern __shared__ __attribute__((aligned(16))) char smem[];
  lptr lds = (lptr)smem;
  u64 pc0;
  asm volatile("s_getpc_b64 %0" : "=s"(pc0));
  const char* code = (const char*)(pc0 & ~(u64)127);
  const int wid = __builtin_amdgcn_readfirstlane(threadIdx.x >> 6), lane = threadIdx.x & 63;
  const int id = blockIdx.x, xcd = id & 7, loc = id >> 3;
  const int rb = 8 * (xcd & 1) + (loc & 7), cg = 4 * (xcd >> 1) + (loc >> 3);
  const int n2t = 4 * xcd + (loc & 3), cb = loc >> 2;
  const int rot = ((loc >> 3) + loc) & 3;
  if (wid >= 6) sync_role(lds, (const char*)wr, (const char*)wi, (const char*)w1, (const char*)w2, ws, code, id, rb, cg, n2t, lane);
  else if (wid >= 4) dma_role(ws, lds, (const char*)x, (const char*)w1, (const char*)w2, id, rb, cg, cb, rot, wid - 4, lane);
  else compute_role(lds, x, ws, out, id, rb, cg, n2t, cb, rot, wid, lane);
}

extern "C" void kernel_launch(void* const* d_in, const int* in_sizes, int n_in, void* d_out, int out_size, void* d_ws, size_t ws_size, hipStream_t stream) {
  hipFuncSetAttribute((const void*)fused_k, hipFuncAttributeMaxDynamicSharedMemorySize, LDS_BYTES);
  fused_k<<<256, 448, LDS_BYTES, stream>>>((const uint16_t*)d_in[0], (const uint16_t*)d_in[1], (const uint16_t*)d_in[2], (const uint16_t*)d_in[4], (const uint16_t*)d_in[5],
                                           (char*)d_ws, (uint16_t*)d_out);
}
```

```cpp
#include <hip/hip_runtime.h>
#include <stdint.h>
#include <utility>

#define DI __device__ __forceinline__
typedef __attribute__((ext_vector_type(8))) __bf16 bf16x8;
typedef __attribute__((ext_vector_type(4))) __bf16 bf16x4;
typedef __attribute__((ext_vector_type(2))) __bf16 bf16x2;
typedef __attribute__((ext_vector_type(4))) float f32x4;
typedef __attribute__((ext_vector_type(2))) float f32x2;
typedef __attribute__((ext_vector_type(4))) short s16x4;
typedef __attribute__((ext_vector_type(8))) short s16x8;
typedef __attribute__((ext_vector_type(4))) unsigned u32x4;
typedef __attribute__((ext_vector_type(2))) unsigned u32x2;
typedef unsigned long long u64;
#define LAS __attribute__((address_space(3)))
typedef LAS char* lptr;
typedef __amdgpu_buffer_rsrc_t rsrc_t;

template <class F, int... I> DI void sfor_impl(F&& f, std::integer_sequence<int, I...>) { (f(std::integral_constant<int, I>{}), ...); }
template <int N, class F> DI void sfor(F&& f) { sfor_impl(f, std::make_integer_sequence<int, N>{}); }

constexpr double kPi = 3.14159265358979323846264338327950288;
constexpr double c_cos(double x) { double x2 = x * x, term = 1.0, sum = 1.0; for (int i = 1; i < 18; ++i) { term *= -x2 / ((2 * i - 1) * (2 * i)); sum += term; } return sum; }
constexpr uint16_t c_f2bf(float f) { uint32_t u = __builtin_bit_cast(uint32_t, f); u += 0x7fffu + ((u >> 16) & 1u); return (uint16_t)(u >> 16); }
struct Cos2048 { float v[2048]; constexpr Cos2048() : v{} { for (int m = 0; m < 2048; ++m) { int mm = m <= 1024 ? m : 2048 - m; double c = c_cos(2.0 * kPi * mm / 2048.0); if (mm == 512) c = 0.0; v[m] = (float)c; } } };
constexpr Cos2048 C2K{};
struct Cos512 { uint16_t v[512]; constexpr Cos512() : v{} { for (int m = 0; m < 512; ++m) v[m] = c_f2bf(C2K.v[4 * m]); } };
constexpr Cos512 C512{};
constexpr uint16_t fval(int d, int c) { const int T = c >> 4, j = 16 * (T >> 1) + (c & 15); const int m = (T & 1) == 0 ? ((d * j) & 511) : (j == 0 ? (d & 1) * 256 : ((d * j + 128) & 511)); return C512.v[m]; }
constexpr int c_sl(int g, int e) { return 16 * (e >> 2) + 4 * g + (e & 3); }
constexpr int c_rho(int v, int t, int li) { return 32 * v + 8 * (li >> 2) + 4 * t + (li & 3); }

template <int P> struct FPart { uint16_t v[16384];
  constexpr FPart() : v{} { for (int cq = 0; cq < 4; ++cq) for (int t = 0; t < 2; ++t) for (int k = 0; k < 4; ++k) for (int l = 0; l < 64; ++l) for (int e = 0; e < 8; ++e)
    v[(((cq * 2 + t) * 4 + k) * 64 + l) * 8 + e] = fval(32 * k + 8 * (l >> 4) + e, 32 * (4 * P + cq) + 16 * t + (l & 15)); } };
template <int P> struct GPart { uint16_t v[16384];
  constexpr GPart() : v{} { for (int s = 0; s < 4; ++s) for (int t = 0; t < 4; ++t) for (int k = 0; k < 2; ++k) for (int l = 0; l < 64; ++l) for (int e = 0; e < 8; ++e)
    v[(((s * 4 + t) * 2 + k) * 64 + l) * 8 + e] = fval(64 * (P >> 1) + 16 * t + (l & 15), 64 * (4 * (P & 1) + s) + 32 * k + 8 * (l >> 4) + e); } };
struct FFTC { uint16_t c64[8192]; float tw1[4096]; uint16_t c32[4096]; float tw3[4096];
  constexpr FFTC() : c64{}, tw1{}, c32{}, tw3{} {
    for (int l = 0; l < 64; ++l) { const int g = l >> 4, li = l & 15;
      for (int p = 0; p < 2; ++p) {
        for (int e = 0; e < 8; ++e) {
          for (int v = 0; v < 2; ++v) for (int t = 0; t < 2; ++t) for (int ks = 0; ks < 2; ++ks)
            c64[((((v * 2 + t) * 2 + ks) * 2 + p) * 64 + l) * 8 + e] = C512.v[(8 * c_rho(v, t, li) * (32 * ks + c_sl(g, e)) + 128 * p) & 511];
          for (int t = 0; t < 2; ++t) c32[((t * 2 + p) * 64 + l) * 8 + e] = C512.v[(16 * (16 * t + li) * c_sl(g, e) + 128 * p) & 511];
        }
        for (int w = 0; w < 4; ++w) for (int t = 0; t < 2; ++t) for (int r = 0; r < 4; ++r) {
          tw1[(((w * 2 + t) * 2 + p) * 64 + l) * 4 + r] = C2K.v[((16 * (w & 1) + 4 * g + r) * c_rho(w >> 1, t, li) + 1536 * p) & 2047];
          tw3[(((w * 2 + t) * 2 + p) * 64 + l) * 4 + r] = C2K.v[((16 * w + 4 * g + r) * (16 * t + li) + 1536 * p) & 2047];
        } } } } };
struct FBig { FPart<0> a0; FPart<1> a1; FPart<2> a2; FPart<3> a3; };
struct GBig { GPart<0> a0; GPart<1> a1; GPart<2> a2; GPart<3> a3; GPart<4> a4; GPart<5> a5; GPart<6> a6; GPart<7> a7; GPart<8> a8; GPart<9> a9; GPart<10> a10; GPart<11> a11; GPart<12> a12; GPart<13> a13; GPart<14> a14; GPart<15> a15; };
#ifdef __HIP_DEVICE_COMPILE__
#define FP(n) constexpr FPart<n> fp##n{};
FP(0) FP(1) FP(2) FP(3)
#define GP(n) constexpr GPart<n> gp##n{};
GP(0) GP(1) GP(2) GP(3) GP(4) GP(5) GP(6) GP(7) GP(8) GP(9) GP(10) GP(11) GP(12) GP(13) GP(14) GP(15)
__device__ const FBig d_FT = {fp0, fp1, fp2, fp3};
__device__ const GBig d_GT = {gp0, gp1, gp2, gp3, gp4, gp5, gp6, gp7, gp8, gp9, gp10, gp11, gp12, gp13, gp14, gp15};
__device__ const FFTC d_FC = FFTC();
#else
__device__ const uint16_t d_FT[4 * 16384] = {0};
__device__ const uint16_t d_GT[16 * 16384] = {0};
__device__ const uint16_t d_FC[28672] = {0};
#endif

DI f32x4 mfma(bf16x8 a, bf16x8 b, f32x4 c) { return __builtin_amdgcn_mfma_f32_16x16x32_bf16(a, b, c, 0, 0, 0); }
DI bf16x8 lds_b128(lptr p) { return *(LAS bf16x8*)p; }
DI f32x4 lds_f4(lptr p) { return *(LAS f32x4*)p; }
DI s16x4 lds_tr(lptr p) { return __builtin_amdgcn_ds_read_tr16_b64_v4i16((LAS s16x4*)p); }
DI bf16x8 lds_tr2(lptr p) { s16x4 a = lds_tr(p), b = lds_tr(p + 512); s16x8 v = __builtin_shufflevector(a, b, 0, 1, 2, 3, 4, 5, 6, 7); return __builtin_bit_cast(bf16x8, v); }
DI void glds16(const void* g, lptr l) { __builtin_amdgcn_global_load_lds((const unsigned*)g, (LAS unsigned*)l, 16, 0, 0); }
template <int N> DI void wait_vm() { asm volatile("s_waitcnt vmcnt(%0)" ::"n"(N) : "memory"); }
DI void wait_lgkm0() { __builtin_amdgcn_s_waitcnt(0xc07f); }
DI void sbar() { __builtin_amdgcn_sched_barrier(0); }
DI void bar() { asm volatile("" ::: "memory"); sbar(); __builtin_amdgcn_s_barrier(); sbar(); asm volatile("" ::: "memory"); }
DI float dot2(unsigned a, unsigned b, float c) { return __builtin_amdgcn_fdot2_f32_bf16(__builtin_bit_cast(bf16x2, a), __builtin_bit_cast(bf16x2, b), c, false); }
DI float bf_round(float f) { return (float)(__bf16)f; }
DI float bflo(unsigned u) { return __builtin_bit_cast(float, u << 16); }
DI float bfhi(unsigned u) { return __builtin_bit_cast(float, u & 0xffff0000u); }
DI bf16x8 pack8(f32x4 a, f32x4 b) { bf16x8 r; r[0] = (__bf16)a[0]; r[1] = (__bf16)a[1]; r[2] = (__bf16)a[2]; r[3] = (__bf16)a[3]; r[4] = (__bf16)b[0]; r[5] = (__bf16)b[1]; r[6] = (__bf16)b[2]; r[7] = (__bf16)b[3]; return r; }
DI bf16x4 pack4(f32x4 a) { bf16x4 r; r[0] = (__bf16)a[0]; r[1] = (__bf16)a[1]; r[2] = (__bf16)a[2]; r[3] = (__bf16)a[3]; return r; }
DI bf16x8 neg8(bf16x8 a) { u32x4 u = __builtin_bit_cast(u32x4, a); u ^= 0x80008000u; return __builtin_bit_cast(bf16x8, u); }
DI float sum_g(float v) {
  unsigned u = __builtin_bit_cast(unsigned, v);
  auto a = __builtin_amdgcn_permlane32_swap(u, u, false, false);
  v = __builtin_bit_cast(float, (unsigned)a[0]) + __builtin_bit_cast(float, (unsigned)a[1]);
  u = __builtin_bit_cast(unsigned, v);
  auto b = __builtin_amdgcn_permlane16_swap(u, u, false, false);
  return __builtin_bit_cast(float, (unsigned)b[0]) + __builtin_bit_cast(float, (unsigned)b[1]);
}
DI float bperm(float v, int src_lane) { return __builtin_bit_cast(float, __builtin_amdgcn_ds_bpermute(src_lane * 4, __builtin_bit_cast(int, v))); }
DI u32x4 ld16(rsrc_t r, int voff, int soff) { return __builtin_amdgcn_raw_buffer_load_b128(r, voff, soff, 16); }
DI void st16(u32x4 v, rsrc_t r, int voff, int soff) { __builtin_amdgcn_raw_buffer_store_b128(v, r, voff + soff, 0, 16); }
DI void st_flag(u32x2 ep, rsrc_t r, int off) { __builtin_amdgcn_raw_buffer_store_b64(ep, r, off, 0, 16); }

constexpr int S_ = 2048, D_ = 512, DFF = 1792;
constexpr int WS_E = 0, WS_F1 = 4096, WS_F2 = 8192, WS_F3 = 12288, WS_DBG = 16384;
constexpr int WS_AT = 131072;
constexpr int WS_WT = WS_AT + 256 * 8192;
constexpr int WS_WS = WS_WT + 256 * 8192;
constexpr int WS_H = WS_WS + 32768;
constexpr int WS_BT = WS_H + 128 * 56 * 1024;
constexpr int WS_END = WS_BT + 512 * 4096;
constexpr int UNIT = 8192, RING = 16 * UNIT, L_Y = RING, Y_P = 96, Y_IM = 64 * Y_P, L_V = L_Y + 2 * Y_IM, V_P = 144, V_IM = 32 * V_P, L_CS = L_V + 2 * V_IM, LDS_BYTES = L_CS + 512;
constexpr int U_X = 1, U_W1 = 17, U_FC = 33, U_W2 = 40, U_G = 68, U_END = 76;
constexpr int NB = 26;
constexpr int c_min(int a, int b) { return a < b ? a : b; }
constexpr int c_need(int b) { return b < 4 ? 5 + 4 * b : b < 12 ? 19 + 2 * (b - 4) : b == 12 ? 37 : b < 15 ? 40 : b < 22 ? c_min(44 + 4 * (b - 15), 68) : b < 24 ? 68 : 76; }
constexpr int c_free(int b) { return b <= 0 ? 0 : b < 5 ? 1 + 4 * b : b < 13 ? 19 + 2 * (b - 5) : b == 13 ? 37 : b < 16 ? 40 : b < 22 ? 44 + 4 * (b - 16) : 68; }
#define W_EARLY 6
#define B_EARLY 8
#define U_CAP 4
constexpr int U_R0 = 56, U_R1 = 68;
#define CX 1
#define H_TAB 1
#define H_M1 9
#define H_0 13
#define H_1 17
#define H_2 21
#define H_3 25
#define CF 1
constexpr bool c_isc(int u) { return u >= (CF ? 0 : U_X) && u < U_X + 4 * CX; }
constexpr int c_hi(int b) {
  int hi = c_min(c_free(b) + 16, U_END);
  if (H_TAB && b < 4) hi = c_min(hi, b == -1 ? H_M1 : b == 0 ? H_0 : b == 1 ? H_1 : b == 2 ? H_2 : H_3);
  else if (b < B_EARLY) hi = c_min(hi, c_need(b + 1) + W_EARLY);
  else if (b < 15) hi = c_min(hi, c_hi(b - 1) + U_CAP);
  return hi;
}
#define B_R0 9
#define R_PER 2
constexpr int c_rhi(int b) { int n = b < B_R0 ? 0 : (b - B_R0 + 1) * R_PER; return n > U_R1 - U_R0 ? U_R1 - U_R0 : n; }
constexpr bool c_isr(int u) { return u >= U_R0 && u < U_R1; }
constexpr int c_after(int b) {
  const int need = c_need(b);
  int u = need - 1;
  while (u >= 0 && (c_isr(u) || c_isc(u))) --u;
  if (u < 0) return 63;
  int bu = -1;
  while (c_hi(bu) <= u) ++bu;
  int n = 0;
  for (int v = u + 1; v < c_hi(b - 1); ++v) if (!c_isr(v) && !c_isc(v)) n += 4;
  n += 4 * (c_rhi(b - 1) - c_rhi(bu - 1));
  return n > 63 ? 63 : n;
}
constexpr int SPIN = 4000;
#define CODE_PF_V 32000
constexpr int CODE_PF = CODE_PF_V;
#define MAP2 1
#define B_W1PF 99
#define B_W1PG 99
#define B_POST_F1 8
#define B_CONF_F1 11
#define B_POST_F3 17
constexpr int B_CONF_F3 = 22;
#define B_GATH 4
#define B_PF2 5
#define PF_PER 2
#define G_PER 2
#define B_FILT 7
#define B_POST_W 9
constexpr int B_CONF_W = 12, WS_FW = WS_F1 + 2048;
#define B_CONF_F2B 17
#define H_PRE 2
#define TLB_T 0
#define KA_PF 1
#define OUT_AUX 0
#define B_SPEC 4
#define PF_N 7
constexpr int c_pf(int b) { int n = b < B_PF2 ? 0 : (b - B_PF2 + 1) * PF_PER; return n > PF_N ? PF_N : n; }
constexpr int B_POST_F2A = 12, B_CONF_F2A = 14, B_POST_F2B = 14;

#define TS_DECL
#define TS(i) do { } while (0)
#define TS_OUT(role) do { } while (0)

template <int N> DI void poll_flags(rsrc_t r, int voff, u32x2 ep) {
#pragma unroll 1
  for (int it = 0; it < SPIN; ++it) {
    bool ok;
    if constexpr (N == 0) { u32x2 v = __builtin_amdgcn_raw_buffer_load_b64(r, voff, 0, 16); ok = v[0] == ep[0] && v[1] == ep[1]; }
    else { ok = true;
#pragma unroll
      for (int k = 0; k < N; ++k) { u32x4 v = __builtin_amdgcn_raw_buffer_load_b128(r, voff + 16 * k, 0, 16); ok = ok && v[0] == ep[0] && v[1] == ep[1] && v[2] == ep[0] && v[3] == ep[1]; } }
    if (__builtin_amdgcn_ballot_w64(!ok) == 0) break;
    __builtin_amdgcn_s_sleep(1);
    asm volatile("" ::: "memory");
  }
  asm volatile("" ::: "memory");
}
DI rsrc_t mk_rsrc(const void* p, int bytes) { return __builtin_amdgcn_make_buffer_rsrc((void*)p, 0, bytes, 0x00020000); }
DI unsigned pf(rsrc_t r, int voff) { return __builtin_amdgcn_raw_buffer_load_b32(r, voff, 0, 0); }
DI unsigned ldu16(rsrc_t r, int voff) { return __builtin_amdgcn_raw_buffer_load_b16(r, voff, 0, 0); }
DI void sink(unsigned v) { asm volatile("" ::"v"(v)); }
template <class T> DI T opq(T v) { asm volatile("" : "+v"(v)); return v; }
template <class T> DI void pin(T& v) { asm volatile("" : "+v"(v)); }

DI void dma4(rsrc_t r, lptr dst, int v0, int v1, int v2, int v3, int so) {
  __builtin_amdgcn_raw_ptr_buffer_load_lds(r, (LAS void*)dst, 16, v0, so, 0, 0);
  __builtin_amdgcn_raw_ptr_buffer_load_lds(r, (LAS void*)dst, 16, v1, so, 1024, 0);
  __builtin_amdgcn_raw_ptr_buffer_load_lds(r, (LAS void*)dst, 16, v2, so, 2048, 0);
  __builtin_amdgcn_raw_ptr_buffer_load_lds(r, (LAS void*)dst, 16, v3, so, 3072, 0);
}
DI void dma4c(rsrc_t r, lptr dst, int v0, int v1, int v2, int v3, int so) {
  __builtin_amdgcn_raw_ptr_buffer_load_lds(r, (LAS void*)dst, 16, v0, so, 0, 16);
  __builtin_amdgcn_raw_ptr_buffer_load_lds(r, (LAS void*)dst, 16, v1, so, 1024, 16);
  __builtin_amdgcn_raw_ptr_buffer_load_lds(r, (LAS void*)dst, 16, v2, so, 2048, 16);
  __builtin_amdgcn_raw_ptr_buffer_load_lds(r, (LAS void*)dst, 16, v3, so, 3072, 16);
}
constexpr int c_w2row(int ks, int hb) { return 128 * (ks >> 1) + 64 * hb + 32 * (ks & 1); }

DI void dma_role(char* ws, lptr lds, const char* x, const char* w1, const char* w2, int id, int rb, int cg, int n2t, int cb, int rot, int dw, int lane) {
  TS_DECL;
  TS(62);
  const int e = lane >> 3, cp = lane & 7;
  const int vlin = lane * 16;
  const int vx = (lane >> 4) * 32768 + ((lane & 15) ^ (lane >> 4)) * 16;
  const int vch = (((cp >> 1) ^ ((e >> 1) & 1)) * 32) + (cp & 1) * 16;
  const int vw1 = e * 3584 + vch, vw2 = (16 * (e >> 2) + (e & 3)) * 1024 + vch;
  const int x1 = vx ^ 64, x2 = vx ^ 128, x3 = vx ^ 192;
  const int a1 = (vw1 ^ 64) + 27648, a2 = vw1 + 2 * 27648, a3 = (vw1 ^ 64) + 3 * 27648;
  const int b1 = (vw2 ^ 64) + 3072, b2 = vw2 + 2 * 3072, b3 = (vw2 ^ 64) + 3 * 3072;
  const rsrc_t rX = mk_rsrc(x, S_ * D_ * 2), rW1 = mk_rsrc(w1, D_ * DFF * 2), rW2 = mk_rsrc(w2, D_ * DFF * 2);
  const rsrc_t rF = mk_rsrc(&d_FT, 131072), rC = mk_rsrc(&d_FC, 57344), rG = mk_rsrc(&d_GT, 524288);
  const int cgw = cg < 14 ? cg : 13;
  const int sx = (32 * (32 * (rb & 1) + 16 * dw) + 4 * (rb >> 1)) * 1024;
  const int sw1 = 32 * dw * 3584 + cgw * 256, sw2 = cb * 128;
  const int sf = cg * 8192 + dw * 4096, sc = dw * 4096, sg = cb * 65536 + dw * 4096;
  lptr ld = lds + dw * 4096;
  auto issue = [&](auto U) {
    constexpr int u = U;
    lptr dst = ld + (u & 15) * UNIT;
    if constexpr (u < U_X) dma4(rF, dst, vlin, vlin, vlin, vlin, sf);
    else if constexpr (u < U_W1) { constexpr int st = (u - U_X) >> 2, w = (u - U_X) & 3; dma4(rX, dst, vx, x1, x2, x3, sx + w * 131072 + ((st + rot) & 3) * 256); }
    else if constexpr (u < U_FC) { constexpr int q = u - U_W1; dma4(rW1, dst, vw1, a1, a2, a3, sw1 + (((q & 7) + 2 * rot) & 7) * (64 * 3584) + (q >> 3) * 128); }
    else if constexpr (u < U_W2) dma4(rC, dst, vlin, vlin, vlin, vlin, sc + (u - U_FC) * 8192);
    else if constexpr (u < U_G) {
      constexpr int q = u - U_W2, hb = q / 14, i = (q % 14) >> 1, p = q & 1;
      dma4(rW2, dst, vw2, b1, b2, b3, sw2 + (dw ? c_w2row(7 * (2 * p + 1) + i, hb) : c_w2row(7 * (2 * p) + i, hb)) * 1024);
    }
    else dma4(rG, dst, vlin, vlin, vlin, vlin, sg + (u - U_G) * 8192);
  };
  u32x4 rg[U_R1 - U_R0][4];
  auto rload = [&](auto U) {
    constexpr int u = U, q = u - U_W2, hb = q / 14, i = (q % 14) >> 1, p = q & 1;
    const int so = sw2 + (dw ? c_w2row(7 * (2 * p + 1) + i, hb) : c_w2row(7 * (2 * p) + i, hb)) * 1024;
    rg[u - U_R0][0] = __builtin_amdgcn_raw_buffer_load_b128(rW2, vw2, so, 0);
    rg[u - U_R0][1] = __builtin_amdgcn_raw_buffer_load_b128(rW2, b1 + 1024, so, 0);
    rg[u - U_R0][2] = __builtin_amdgcn_raw_buffer_load_b128(rW2, b2 + 2048, so, 0);
    rg[u - U_R0][3] = __builtin_amdgcn_raw_buffer_load_b128(rW2, b3 + 3072, so, 0);
  };
  auto place = [&](auto U) {
    constexpr int u = U;
    if constexpr (c_isr(u)) {
#pragma unroll
      for (int i = 0; i < 4; ++i) *(LAS u32x4*)(ld + (u & 15) * UNIT + i * 1024 + lane * 16) = rg[u - U_R0][i];
    } else if constexpr (!c_isc(u)) issue(U);
  };
  sfor<c_hi(-1)>([&](auto U) { if constexpr (!c_isc(U)) issue(U); });
  TS(63);
  sfor<NB>([&](auto B) {
    constexpr int b = B;
    constexpr int issued = c_hi(b - 1);
    static_assert(issued >= c_need(b), "schedule");
    wait_vm<(b == 24 ? 16 : b == 25 ? 0 : c_after(b))>();
    wait_lgkm0();
    TS(b);
    bar();
    if constexpr (b == 22 || b == 23) {
      const rsrc_t rB = mk_rsrc(ws, WS_END);
      const int l2 = opq(lane), vb = (l2 >> 3) * 4096 + ((((l2 & 7) >> 1) ^ ((l2 >> 4) & 1)) * 32) + (l2 & 1) * 16;
#pragma unroll
      for (int q = 0; q < 4; ++q) {
        const int t = b == 22 ? 4 + q : q;
        dma4c(rB, ld + ((U_END + t) & 15) * UNIT, vb, (vb ^ 64) + 31744, vb + 2 * 31744, (vb ^ 64) + 3 * 31744, WS_BT + (64 * t + 32 * dw) * 4096 + n2t * 128);
      }
    }
    constexpr int hi = c_hi(b);
    static_assert(hi >= issued, "schedule");
    sfor<hi - issued>([&](auto I) { place(std::integral_constant<int, issued + I>{}); });
    sbar();
    sfor<c_rhi(b) - c_rhi(b - 1)>([&](auto I) { rload(std::integral_constant<int, U_R0 + c_rhi(b - 1) + I>{}); });
    sbar();
    if constexpr (b == 13) { if (id == 0) { bar(); bar(); bar(); } }
  });
  if (dw == 0) TS_OUT(1);
}

typedef unsigned u32a2 __attribute__((aligned(2)));
DI void sync_role(lptr lds, const char* __restrict__ wr, const char* __restrict__ wi, const char* w1, const char* w2, char* ws, const char* code, const char* outp, int id, int rb, int cg, int n2t, int lane) {
  TS_DECL;
  TS(62);
  const rsrc_t rs = mk_rsrc(ws, WS_END);
  const int xcd = id & 7, loc = id >> 3;
  const u32x2 e0 = __builtin_amdgcn_raw_buffer_load_b64(rs, WS_E, 0, 16);
  unsigned p0[4], p1[9];
  [[maybe_unused]] unsigned pw[2] = {0, 0};
  p0[1] = p0[2] = 0;
  { const rsrc_t rI = mk_rsrc(code, CODE_PF), rC = mk_rsrc(&d_FC, 57344);
    p0[0] = pf(rI, ((loc & 3) * 64 + lane) * 128);
    p0[3] = pf(rC, lane < 14 ? (loc * 14 + lane) * 128 : 0x7ffffff0); }
  u64 E = ((u64)(unsigned)__builtin_amdgcn_readfirstlane(e0[1]) << 32) | (unsigned)__builtin_amdgcn_readfirstlane(e0[0]);
  E = E * 6364136223846793005ull + 1442695040888963407ull;
  const u32x2 ep = {(unsigned)E, (unsigned)(E >> 32)};
  TS(63);
  const int wk = rb >> 2, gk = rb & 3;
  unsigned fa[4][4], fb[4][4], sp[2][8];
  u32x2 q1; u32x4 q3[2]; bool ok1 = false, ok3 = false;
  sfor<NB>([&](auto B) {
    constexpr int b = B;
    if constexpr (b == B_CONF_F1) { if (!ok1) poll_flags<0>(rs, WS_F1 + ((id >> 4) * 16 + (lane & 15)) * 8, ep); }
    if constexpr (b == B_CONF_F2A) poll_flags<0>(rs, WS_F2 + ((2 * (n2t >> 2)) * 16 + (lane & 31)) * 8, ep);
    if constexpr (b == B_CONF_F2B) poll_flags<0>(rs, WS_F2 + 2048 + ((2 * (n2t >> 2)) * 16 + (lane & 31)) * 8, ep);
    if constexpr (b == B_CONF_F3) { if (!ok3) poll_flags<2>(rs, WS_F3 + lane * 32, ep); }
    if constexpr (b == B_CONF_W) poll_flags<0>(rs, WS_FW + ((id >> 4) * 16 + (lane & 15)) * 8, ep);
    if constexpr (b == B_POST_W) wait_vm<c_pf(B_POST_W - 1) - c_pf(B_FILT)>();
    if constexpr (b == B_POST_F3) wait_vm<0>();
    TS(b);
    bar();
    TS(31 + b);
    if constexpr (b == B_POST_F1) { if (lane == 0) st_flag(ep, rs, WS_F1 + (cg * 16 + rb) * 8); }
    if constexpr (b == B_POST_W) { if (lane == 0) st_flag(ep, rs, WS_FW + (cg * 16 + rb) * 8); }
    if constexpr (b == B_POST_F2A) { if (lane == 0) st_flag(ep, rs, WS_F2 + (rb * 16 + cg) * 8); }
    if constexpr (b == B_POST_F2B) { if (lane == 0) st_flag(ep, rs, WS_F2 + 2048 + (rb * 16 + cg) * 8); }
    if constexpr (b == B_POST_F3) { if (lane == 0) st_flag(ep, rs, WS_F3 + id * 8); }
    if constexpr (b == B_CONF_F3) { if (id == 0 && lane == 0) st_flag(ep, rs, WS_E); }
    if constexpr (b == B_CONF_F1 - 1) ok1 = __builtin_amdgcn_ballot_w64(!(q1[0] == ep[0] && q1[1] == ep[1])) == 0;
    if constexpr (b == B_CONF_F3 - 1)
      ok3 = __builtin_amdgcn_ballot_w64(!(q3[0][0] == ep[0] && q3[0][1] == ep[1] && q3[0][2] == ep[0] && q3[0][3] == ep[1] && q3[1][0] == ep[0] && q3[1][1] == ep[1] && q3[1][2] == ep[0] && q3[1][3] == ep[1])) == 0;
    if constexpr (b == B_W1PF || b == B_W1PG) {
      const rsrc_t rW1 = mk_rsrc(w1, D_ * DFF * 2);
      const int col = (xcd >> 2) * 2048 + (lane & 15) * 128, o = col < 3584 ? (16 * loc + (lane >> 4)) * 3584 + col : 0x7ffffff0 - 12 * 3584;
      if constexpr (b == B_W1PF) { p0[1] = pf(rW1, o); p0[2] = pf(rW1, o + 4 * 3584); }
      if constexpr (b == B_W1PG) { pw[0] = pf(rW1, o + 8 * 3584); pw[1] = pf(rW1, o + 12 * 3584); }
    }
    if constexpr (b >= B_GATH && b < B_GATH + 4 / G_PER) {
#pragma unroll
      for (int R = G_PER * (b - B_GATH); R < G_PER * (b - B_GATH) + G_PER; ++R)
#pragma unroll
        for (int r = 0; r < 4; ++r) {
          const int k = 16 * wk + 8 * (R >> 1) + (lane >> 3) + 64 * (16 * (R & 1) + 4 * gk + r), o = k * 514 + (16 * cg + 2 * (lane & 7)) * 2;
          fa[R][r] = *(const u32a2*)(wr + o); fb[R][r] = *(const u32a2*)(wi + o);
        }
      sbar();
    }
    if constexpr (b == B_SPEC) {
      const rsrc_t rR = mk_rsrc(wr, cg == 0 ? S_ * 257 * 2 : 0), rI = mk_rsrc(wi, cg == 0 ? S_ * 257 * 2 : 0);
#pragma unroll
      for (int it = 0; it < 2; ++it) {
        const int e = 4 * it + (lane >> 4), l = lane & 15;
        const int k = 16 * wk + l + 64 * (16 * (e >> 2) + 4 * gk + (e & 3)), km = (2048 - k) & 2047;
        sp[it][0] = ldu16(rR, k * 514); sp[it][1] = ldu16(rI, k * 514); sp[it][2] = ldu16(rR, km * 514); sp[it][3] = ldu16(rI, km * 514);
        sp[it][4] = ldu16(rR, k * 514 + 512); sp[it][5] = ldu16(rI, k * 514 + 512); sp[it][6] = ldu16(rR, km * 514 + 512); sp[it][7] = ldu16(rI, km * 514 + 512);
      }
      sbar();
    }
    if constexpr (b == B_FILT) {
      sink(p0[0]); sink(p0[1]); sink(p0[2]); sink(p0[3]);
      sink(pw[0]); sink(pw[1]);
#pragma unroll
      for (int R = 0; R < 4; ++R) {
        u32x4 v0, v1;
#pragma unroll
        for (int r = 0; r < 4; ++r) { v0[r] = (fa[R][r] & 0xffffu) | (fb[R][r] << 16); v1[r] = (fa[R][r] >> 16) | (fb[R][r] & 0xffff0000u); }
        const int wo = WS_WT + (16 * cg + 2 * (lane & 7)) * 8192 + wk * 2048 + (R & 1) * 1024 + (16 * gk + 8 * (R >> 1) + (lane >> 3)) * 16;
        st16(v0, rs, wo, 0); st16(v1, rs, wo + 8192, 0);
      }
    }
    if constexpr (b == B_FILT) {
#pragma unroll
      for (int it = 0; it < 2; ++it) {
        const int e = 4 * it + (lane >> 4), l = lane & 15;
        const float w0r = bflo(sp[it][0]), w0i = bflo(sp[it][1]), m0r = bflo(sp[it][2]), m0i = bflo(sp[it][3]);
        const float wnr = bflo(sp[it][4]), wni = bflo(sp[it][5]), mnr = bflo(sp[it][6]), mni = bflo(sp[it][7]);
        const float ar = 0.5f * (w0r + m0r), ai = 0.5f * (w0i - m0i), br = 0.5f * (wnr + mnr), bi = 0.5f * (wni - mni);
        const f32x4 uv = {0.25f * (ar + br), 0.25f * (ai + bi), 0.25f * (ar - br), 0.25f * (ai - bi)};
        st16(__builtin_bit_cast(u32x4, uv), rs, cg == 0 ? WS_WS + ((wk * 8 + e) * 64 + 16 * gk + l) * 16 : 0x7f000000, 0);
      }
    }
    if constexpr (c_pf(b) > c_pf(b - 1)) {
      const rsrc_t rW2 = mk_rsrc(w2, D_ * DFF * 2), rG = mk_rsrc(&d_GT, 524288);
#pragma unroll
      for (int i = c_pf(b - 1); i < c_pf(b); ++i) p1[i] = i < 7 ? pf(rW2, loc * 57344 + (i * 64 + lane) * 128) : pf(rG, loc * 16384 + ((i - 7) * 64 + lane) * 128);
      sbar();
    }
    if constexpr (b == B_CONF_F1 - 2) { q1 = __builtin_amdgcn_raw_buffer_load_b64(rs, WS_F1 + ((id >> 4) * 16 + (lane & 15)) * 8, 0, 16); sbar(); }
    if constexpr (b == B_CONF_F3 - 2) { q3[0] = __builtin_amdgcn_raw_buffer_load_b128(rs, WS_F3 + lane * 32, 0, 16); q3[1] = __builtin_amdgcn_raw_buffer_load_b128(rs, WS_F3 + lane * 32 + 16, 0, 16); sbar(); }
    if constexpr (b == 12) {
#pragma unroll
      for (int i = 0; i < PF_N; ++i) sink(p1[i]);
    }
    if constexpr (b == 13) { if (id == 0) { bar(); bar(); bar(); } }
    if constexpr (b == 15) {
      const int cre = 32 * (id >> 4) + (id & 15);
      const int bo = WS_BT + cre * 4096 + (lane >> 3) * 128 + (((lane & 7) ^ (lane >> 3)) * 16);
      u32x4 v[8];
#pragma unroll
      for (int i = 0; i < 8; ++i) v[i] = *(LAS u32x4*)(lds + L_Y + i * 1024 + lane * 16);
#pragma unroll
      for (int i = 0; i < 8; ++i) st16(v[i], rs, bo + (i >> 2) * 65536 + (i & 3) * 1024, 0);
      wait_lgkm0();
    }
  });
  TS_OUT(2);
}
DI void compute_role(lptr lds, const uint16_t* __restrict__ x, char* ws, uint16_t* __restrict__ out, int id, int rb, int cg, int n2t, int cb, int rot, int w, int lane, u64 tent_) {
  int g = lane >> 4, li = lane & 15;
  auto fresh = [&]() { lane = opq(lane); g = lane >> 4; li = lane & 15; };
  TS_DECL;
  TS(62);
  [[maybe_unused]] int bi_ = 0;
  auto tbar = [&]() { TS(bi_); bar(); TS(31 + bi_); ++bi_; };
  const rsrc_t rs = mk_rsrc(ws, WS_END);
  const f32x4 z4 = {0.f, 0.f, 0.f, 0.f};
  const bf16x8 ones = __builtin_bit_cast(bf16x8, u32x4{0x3f803f80u, 0x3f803f80u, 0x3f803f80u, 0x3f803f80u});
  bf16x8 xr[2][16];
  f32x4 acc[2][4];
  float mu[2], rsd[2];
  bf16x8 wf[2][4];
  int ta;
  auto rd2 = [&](int buf, int base) {
#pragma unroll
    for (int t = 0; t < 4; ++t) wf[buf][t] = lds_tr2(lds + base + ((ta ^ (w << 5)) ^ (t << 5)));
  };
  {
    f32x4 CS[2][2][2], SU[2] = {z4, z4}, GR[2] = {z4, z4};
    const int xa = li * 256 + ((g ^ li) * 16);
    const unsigned sgn = (unsigned)(li & 1) << 31;
    bf16x8 ff[2][4];
    sfor<4>([&](auto ST) {
      constexpr int st = ST;
      if constexpr (st == 0) {
        { const rsrc_t rF = mk_rsrc(&d_FT, 131072);
          const int sf = cg * 8192 + w * 2048;
          __builtin_amdgcn_raw_ptr_buffer_load_lds(rF, (LAS void*)(lds + w * 2048), 16, lane * 16, sf, 0, 0);
          __builtin_amdgcn_raw_ptr_buffer_load_lds(rF, (LAS void*)(lds + w * 2048), 16, lane * 16, sf, 1024, 0);
          sbar(); }
        const rsrc_t rX = mk_rsrc(x, S_ * D_ * 2);
        const int vx = (lane >> 4) * 32768 + ((lane & 15) ^ (lane >> 4)) * 16;
        const int sx = (32 * (32 * (rb & 1)) + 4 * (rb >> 1)) * 1024 + w * 131072;
        sfor<2 * CX>([&](auto Q) {
          constexpr int q = Q, xs_ = q >> 1, dw = q & 1;
          dma4(rX, lds + dw * 4096 + (U_X + 4 * xs_ + w) * UNIT, vx, vx ^ 64, vx ^ 128, vx ^ 192, sx + dw * (32 * 16 * 1024) + ((xs_ + rot) & 3) * 256);
        });
      }
      if constexpr (st < CX || (CF && st == 0)) wait_vm<(CX > st ? 8 * (CX - 1 - st) : 0)>();
      tbar();
      constexpr int xs0 = (U_X + 4 * st) * UNIT;
      const int xs = (xs0 + w * UNIT) & (RING - 1);
      if constexpr (st == 0) {
#pragma unroll
        for (int k = 0; k < 4; ++k) { ff[0][k] = lds_b128(lds + lane * 16 + k * 1024); ff[1][k] = lds_b128(lds + lane * 16 + 4096 + k * 1024); }
      }
#pragma unroll
      for (int k = 0; k < 4; ++k) { xr[0][4 * st + k] = lds_b128(lds + xs + (xa ^ (k << 6))); xr[1][4 * st + k] = lds_b128(lds + xs + (xa ^ (k << 6)) + 4096); }
#pragma unroll
      for (int m = 0; m < 2; ++m)
#pragma unroll
        for (int p = 0; p < 2; ++p) {
          if constexpr (st < 2) CS[st & 1][m][p] = z4;
          else CS[st & 1][m][p] = __builtin_bit_cast(f32x4, __builtin_bit_cast(u32x4, CS[st & 1][m][p]) ^ sgn);
        }
#pragma unroll
      for (int k = 0; k < 4; ++k)
#pragma unroll
        for (int m = 0; m < 2; ++m) {
          CS[st & 1][m][0] = mfma(xr[m][4 * st + k], ff[0][k], CS[st & 1][m][0]); CS[st & 1][m][1] = mfma(xr[m][4 * st + k], ff[1][k], CS[st & 1][m][1]);
          SU[m] = mfma(ones, xr[m][4 * st + k], SU[m]); GR[m] = mfma(xr[m][4 * st + k], xr[m][4 * st + k], GR[m]);
        }
#pragma unroll
      for (int m = 0; m < 2; ++m) { pin(CS[st & 1][m][0]); pin(CS[st & 1][m][1]); pin(SU[m]); pin(GR[m]); }
      wait_lgkm0();
    });
    fresh();
    ta = (8 * g + (li >> 2)) * 128 + (li & 3) * 8 + ((((li >> 3) & 1) + 2 * (g & 1)) * 32);
    tbar();
    rd2(0, (U_W1 & 15) * UNIT);
    sbar();
#pragma unroll
    for (int m = 0; m < 2; ++m) {
      float dg = (li & 2) ? ((li & 1) ? GR[m][3] : GR[m][2]) : ((li & 1) ? GR[m][1] : GR[m][0]);
      dg = g == (li >> 2) ? dg : 0.f;
      const float s2 = sum_g(dg);
      const float mean = SU[m][0] * (1.f / 512.f);
      const float var = fmaxf(s2 * (1.f / 512.f) - mean * mean, 0.f);
      const float den = bf_round(__builtin_amdgcn_sqrtf(bf_round(bf_round(var) + 1e-5f)));
      mu[m] = bf_round(mean); rsd[m] = __builtin_amdgcn_rcpf(den);
    }
    float sr[2], si[2]; bool sw[2];
#pragma unroll
    for (int p = 0; p < 2; ++p) { const int q = ((li & 3) * ((2 + p + rot) & 3)) & 3; sw[p] = q & 1; sr[p] = q < 2 ? 1.f : -1.f; si[p] = (q == 0 || q == 3) ? 1.f : -1.f; }
    const float csf = (cg == 0 && li == 0) ? 512.f : 0.f;
    f32x4 v[2][2];
#pragma unroll
    for (int m = 0; m < 2; ++m)
#pragma unroll
      for (int r = 0; r < 4; ++r) {
        const float r4 = bperm(rsd[m], 4 * g + r), m4 = bperm(mu[m], 4 * g + r);
        const float re = sr[0] * (sw[0] ? CS[0][m][1][r] : CS[0][m][0][r]) + sr[1] * (sw[1] ? CS[1][m][1][r] : CS[1][m][0][r]);
        const float im = si[0] * (sw[0] ? CS[0][m][0][r] : CS[0][m][1][r]) + si[1] * (sw[1] ? CS[1][m][0][r] : CS[1][m][1][r]);
        v[m][0][r] = (re - m4 * csf) * r4; v[m][1][r] = im * r4;
      }
    const int ao = WS_AT + (16 * cg + li) * 8192 + (rb >> 1) * 512 + (rb & 1) * 256 + w * 64 + g * 16;
    st16(__builtin_bit_cast(u32x4, pack8(v[0][0], v[1][0])), rs, ao, 0);
    st16(__builtin_bit_cast(u32x4, pack8(v[0][1], v[1][1])), rs, ao + 4096, 0);
    sbar();
  }
  const int j = id, pu = w & 1, pv = w >> 1;
  auto h_epilogue = [&](int hb) {
    fresh();
    const float mu_[2] = {opq(mu[0]), opq(mu[1])}, rsd_[2] = {opq(rsd[0]), opq(rsd[1])};
    const bool odd = w & 1;
    const int hoff = cg < 14 ? WS_H + (((4 * (rb >> 1) + (li >> 2)) * 4 + 2 * (rb & 1)) * 56) * 1024 + (16 * g + 4 * w + (li & 3)) * 16 : 0x7f000000;
#pragma unroll
    for (int m = 0; m < 2; ++m)
#pragma unroll
      for (int u = 0; u < 2; ++u) {
        f32x4 v[2];
#pragma unroll
        for (int t = 0; t < 2; ++t) {
          const f32x4 c4 = lds_f4(lds + L_CS + hb * 256 + (((2 * u + t) ^ w) * 64) + g * 16);
          v[t] = (acc[m][2 * u + t] - c4 * mu_[m]) * rsd_[m];
#pragma unroll
          for (int e = 0; e < 4; ++e) v[t][e] = fmaxf(v[t][e], 0.f);
        }
        const u32x2 p0 = __builtin_bit_cast(u32x2, pack4(v[0])), p1 = __builtin_bit_cast(u32x2, pack4(v[1]));
        const u32x4 o = {odd ? p1[0] : p0[0], odd ? p1[1] : p0[1], odd ? p0[0] : p1[0], odd ? p0[1] : p1[1]};
        const int ks56 = hb * 28 + 2 * cg + (u ^ (w >> 1));
        st16(o, rs, hoff + m * 56 * 1024, ks56 * 1024);
      }
  };
  bf16x8 zr[2], zi[2];
  u32x4 wq[2];
  sfor<2>([&](auto HB) {
    constexpr int hb = HB;
#pragma unroll
    for (int m = 0; m < 2; ++m)
#pragma unroll
      for (int t = 0; t < 4; ++t) acc[m][t] = z4;
    f32x4 csa = z4;
    sfor<16>([&](auto KS) {
      constexpr int ks = KS, cur = ks & 1, nxt = cur ^ 1;
      sbar();
      if constexpr (ks < 15) {
        if constexpr ((ks & 3) == 3) {
          constexpr int b = 5 + 4 * hb + (ks >> 2);
          wait_lgkm0();
          if constexpr (b == B_POST_F1) wait_vm<(b > 8 ? 4 : 0)>();
          tbar();
          if constexpr (b == B_CONF_F1) {
            fresh();
            const int zo = WS_AT + j * 8192 + (4 * pu + (li >> 2)) * 512 + g * 64 + (li & 3) * 16;
#pragma unroll
            for (int k = 0; k < 2; ++k) { zr[k] = __builtin_bit_cast(bf16x8, ld16(rs, zo + k * 256, 0)); zi[k] = __builtin_bit_cast(bf16x8, ld16(rs, zo + 4096 + k * 256, 0)); }
            sbar();
          }
        }
        constexpr int un = U_W1 + 8 * hb + ((ks + 1) >> 1);
        rd2(nxt, (un & 15) * UNIT + ((ks + 1) & 1) * 4096);
      }
#pragma unroll
      for (int t = 0; t < 4; ++t)
#pragma unroll
        for (int m = 0; m < 2; ++m) acc[m][t] = mfma(wf[cur][t], xr[m][ks], acc[m][t]);
      csa = mfma(wf[cur][0], ones, csa);
      if constexpr (ks < 15) {
#pragma unroll
        for (int i = 0; i < 8; ++i) { __builtin_amdgcn_sched_group_barrier(0x008, 1, 0); __builtin_amdgcn_sched_group_barrier(0x100, 1, 0); }
        __builtin_amdgcn_sched_group_barrier(0x008, 1, 0);
      }
    });
    sbar();
    if (li == 0) *(LAS f32x4*)(lds + L_CS + hb * 256 + w * 64 + g * 16) = csa;
    wait_lgkm0();
    if constexpr (hb == 0 && B_POST_F1 == 8) wait_vm<0>();
    if constexpr (hb == 1) wait_vm<(B_CONF_F1 <= 11 ? 4 : 0)>();
    tbar();
    if constexpr (hb == 0) rd2(0, ((U_W1 + 8) & 15) * UNIT);
    if constexpr (hb == 1) {
      fresh();
      wq[0] = ld16(rs, WS_WT + j * 8192 + w * 2048 + lane * 16, 0); wq[1] = ld16(rs, WS_WT + j * 8192 + w * 2048 + 1024 + lane * 16, 0);
    }
    sbar();
    h_epilogue(hb);
    sbar();
  });
  bf16x8 hr[4][7];
  fresh();
  const int ho = WS_H + (n2t * 4 * 56 + 7 * w) * 1024 + lane * 16;
  auto ld_h = [&](int i, int half) {
#pragma unroll
    for (int rt = 0; rt < 4; ++rt) hr[rt][i] = __builtin_bit_cast(bf16x8, ld16(rs, ho + i * 1024, ((rt ^ (w & 2)) * 56 + 28 * half) * 1024));
  };
  {
    lptr fc = lds + (U_FC & 15) * UNIT + lane * 16;
    bf16x8 c64[2][2][2];
    {
      f32x4 tw1[2][2];
#pragma unroll
      for (int t = 0; t < 2; ++t)
#pragma unroll
        for (int p = 0; p < 2; ++p) {
#pragma unroll
          for (int ks = 0; ks < 2; ++ks) c64[t][ks][p] = lds_b128(fc + pv * 8192 + ((t * 2 + ks) * 2 + p) * 1024);
          tw1[t][p] = lds_f4(fc + 16384 + w * 4096 + (t * 2 + p) * 1024);
        }
#pragma unroll
      for (int t = 0; t < 2; ++t) {
        f32x4 yr = z4, yi = z4;
#pragma unroll
        for (int ks = 0; ks < 2; ++ks) {
          yr = mfma(zr[ks], c64[t][ks][0], yr); yr = mfma(neg8(zi[ks]), c64[t][ks][1], yr);
          yi = mfma(zi[ks], c64[t][ks][0], yi); yi = mfma(zr[ks], c64[t][ks][1], yi);
        }
        const f32x4 pr = yr * tw1[t][0] + yi * tw1[t][1], pi = yi * tw1[t][0] - yr * tw1[t][1];
        lptr p = lds + L_Y + (32 * pv + 8 * (li >> 2) + 4 * t + (li & 3)) * Y_P + (8 * g + 4 * pu) * 2;
        *(LAS bf16x4*)p = pack4(pr); *(LAS bf16x4*)(p + Y_IM) = pack4(pi);
      }
    }
    wait_lgkm0();
    tbar();
    bf16x8 c32[2][2];
    f32x4 tw3[2][2];
#pragma unroll
    for (int t = 0; t < 2; ++t)
#pragma unroll
      for (int p = 0; p < 2; ++p) { c32[t][p] = lds_b128(fc + 32768 + (t * 2 + p) * 1024); tw3[t][p] = lds_f4(fc + 40960 + w * 4096 + (t * 2 + p) * 1024); }
    f32x4 xre[2], xim[2];
    {
      lptr p = lds + L_Y + (16 * w + li) * Y_P + g * 16;
      const bf16x8 ur = lds_b128(p), ui = lds_b128(p + Y_IM), nui = neg8(ui);
#pragma unroll
      for (int t = 0; t < 2; ++t) {
        xre[t] = mfma(c32[t][0], ur, z4); xre[t] = mfma(c32[t][1], nui, xre[t]);
        xim[t] = mfma(c32[t][1], ur, z4); xim[t] = mfma(c32[t][0], ui, xim[t]);
      }
    }
    f32x4 pr[2], pi[2];
    if (j != 0) {
#pragma unroll
      for (int t = 0; t < 2; ++t)
#pragma unroll
        for (int r = 0; r < 4; ++r) {
          const float wre = bflo(wq[t][r]), wim = bfhi(wq[t][r]);
          pr[t][r] = xre[t][r] * wre - xim[t][r] * wim; pi[t][r] = xre[t][r] * wim + xim[t][r] * wre;
        }
    } else {
      wait_lgkm0();
      bar();
      LAS f32x2* X = (LAS f32x2*)(lds + L_Y);
#pragma unroll
      for (int t = 0; t < 2; ++t)
#pragma unroll
        for (int r = 0; r < 4; ++r) X[16 * w + li + 64 * (16 * t + 4 * g + r)] = f32x2{xre[t][r], xim[t][r]};
      wait_lgkm0();
      bar();
#pragma unroll
      for (int t = 0; t < 2; ++t)
#pragma unroll
        for (int r = 0; r < 4; ++r) {
          const int k = 16 * w + li + 64 * (16 * t + 4 * g + r);
          const f32x2 xm = X[(2048 - k) & 2047];
          const f32x4 c = __builtin_bit_cast(f32x4, ld16(rs, WS_WS + ((w * 8 + 4 * t + r) * 64 + lane) * 16, 0));
          pr[t][r] = xre[t][r] * c[0] - xim[t][r] * c[1] + xm[0] * c[2] + xm[1] * c[3];
          pi[t][r] = xre[t][r] * c[1] + xim[t][r] * c[0] + xm[0] * c[3] - xm[1] * c[2];
        }
      wait_lgkm0();
      bar();
    }
    {
      const bf16x8 Pr = pack8(pr[0], pr[1]), Pi = pack8(pi[0], pi[1]), nPr = neg8(Pr);
#pragma unroll
      for (int t = 0; t < 2; ++t) {
        f32x4 vr = mfma(Pr, c32[t][0], z4); vr = mfma(Pi, c32[t][1], vr);
        f32x4 vi = mfma(Pi, c32[t][0], z4); vi = mfma(nPr, c32[t][1], vi);
        const f32x4 ar = vr * tw3[t][0] - vi * tw3[t][1], ai = vr * tw3[t][1] + vi * tw3[t][0];
        lptr p = lds + L_V + (16 * t + li) * V_P + (32 * (w >> 1) + 8 * g + 4 * (w & 1)) * 2;
        *(LAS bf16x4*)p = pack4(ar); *(LAS bf16x4*)(p + V_IM) = pack4(ai);
      }
    }
    wait_lgkm0();
    wait_vm<0>();
    tbar();
    sbar();
#pragma unroll
    for (int i = 0; i < H_PRE; ++i) ld_h(i, 0);
    sbar();
    {
      bf16x8 vr[2], vi[2];
#pragma unroll
      for (int ks = 0; ks < 2; ++ks) { lptr p = lds + L_V + (16 * pu + li) * V_P + (32 * ks + 8 * g) * 2; vr[ks] = lds_b128(p); vi[ks] = lds_b128(p + V_IM); }
      f32x4 yr[2], yi[2];
#pragma unroll
      for (int t = 0; t < 2; ++t) {
        yr[t] = z4; yi[t] = z4;
#pragma unroll
        for (int ks = 0; ks < 2; ++ks) {
          yr[t] = mfma(c64[t][ks][0], vr[ks], yr[t]); yr[t] = mfma(c64[t][ks][1], vi[ks], yr[t]);
          yi[t] = mfma(c64[t][ks][0], vi[ks], yi[t]); yi[t] = mfma(c64[t][ks][1], neg8(vr[ks]), yi[t]);
        }
      }
      lptr p = lds + L_Y + (16 * pu + li) * 128 + (((4 * pv + g) ^ (li & 7)) * 16);
      *(LAS bf16x8*)p = pack8(yr[0], yr[1]); *(LAS bf16x8*)(p + 4096) = pack8(yi[0], yi[1]);
    }
    sbar();
  }
  f32x4 acc1[4][4];
  {
    fresh();
    ta = (8 * g + (li >> 2)) * 128 + (li & 3) * 8 + ((((li >> 3) & 1) + 2 * (g & 1)) * 32);
    const int tw = (ta ^ ((w & 1) << 6)) + w * 4096;
    wait_lgkm0();
    tbar();
    bf16x8 wg[2][4];
    auto rd4 = [&](int buf, int base) {
#pragma unroll
      for (int t = 0; t < 4; ++t) wg[buf][t] = lds_tr2(lds + base + (tw ^ (t << 5)));
    };
    rd4(0, (U_W2 & 15) * UNIT);
#pragma unroll
    for (int rt = 0; rt < 4; ++rt)
#pragma unroll
      for (int t = 0; t < 4; ++t) acc1[rt][t] = z4;
    sfor<14>([&](auto SG) {
      constexpr int sg = SG, cur = sg & 1, nxt = cur ^ 1, i = sg % 7;
      sbar();
      constexpr int sgc = 2 * (B_CONF_F2B - 16) + 1;
      if constexpr ((sg & 1) && sg < 13) {
        wait_lgkm0();
        tbar();
        if constexpr (sg == sgc) {
#pragma unroll
          for (int q = 0; q < sgc; ++q) ld_h(q, 1);
          sbar();
        }
      }
      if constexpr (sg == 0) { ld_h(H_PRE, 0); if constexpr (H_PRE + 1 < 7) ld_h(H_PRE + 1, 0); if constexpr (H_PRE + 2 < 7) ld_h(H_PRE + 2, 0); sbar(); }
      if constexpr (sg == 1) {
#pragma unroll
        for (int q = H_PRE + 3; q < 7; ++q) ld_h(q, 0);
        sbar();
      }
      if constexpr (sg < 13) rd4(nxt, ((U_W2 + 2 * (sg + 1)) & 15) * UNIT);
#pragma unroll
      for (int t = 0; t < 4; ++t)
#pragma unroll
        for (int rt = 0; rt < 4; ++rt) acc1[rt][t] = mfma(wg[cur][t], hr[rt][i], acc1[rt][t]);
      if constexpr (sg < 13) {
#pragma unroll
        for (int q = 0; q < 8; ++q) { __builtin_amdgcn_sched_group_barrier(0x008, 2, 0); __builtin_amdgcn_sched_group_barrier(0x100, 1, 0); }
      }
      if constexpr (sg >= sgc && sg < 7) { sbar(); ld_h(i, 1); }
    });
    sbar();
  }
  fresh();
  ta = (8 * g + (li >> 2)) * 128 + (li & 3) * 8 + ((((li >> 3) & 1) + 2 * (g & 1)) * 32);
  u32x2 xv[2][2];
  f32x4 gate[2][2], acc2[2][2];
  const int col0 = 64 * cb + 32 * (w & 1);
  {
    auto red = [&](int o, int d) { const int grp = o * 3 + d - 1; return lds + (grp < 8 ? 12 * UNIT + grp * 4096 : L_Y + (grp - 8) * 4096) + lane * 16; };
#pragma unroll
    for (int d = 1; d < 4; ++d) {
      lptr p = red(w ^ d, d);
#pragma unroll
      for (int t = 0; t < 4; ++t) *(LAS f32x4*)(p + t * 1024) = acc1[2 * (d >> 1) + (t >> 1)][2 * (d & 1) + (t & 1)];
    }
#pragma unroll
    for (int a = 0; a < 2; ++a)
#pragma unroll
      for (int c = 0; c < 2; ++c) xv[a][c] = *(const u32x2*)(x + (size_t)(32 * (16 * ((w & 2) + a) + li) + n2t) * 512 + col0 + 16 * c + 4 * g);
    wait_lgkm0();
    tbar();
#pragma unroll
    for (int d = 1; d < 4; ++d) {
      lptr p = red(w, d);
#pragma unroll
      for (int t = 0; t < 4; ++t) acc1[t >> 1][t & 1] += lds_f4(p + t * 1024);
    }
#pragma unroll
    for (int a = 0; a < 2; ++a) { pin(acc1[a][0]); pin(acc1[a][1]); }
    wait_lgkm0();
    tbar();
#pragma unroll
    for (int a = 0; a < 2; ++a)
#pragma unroll
      for (int c = 0; c < 2; ++c)
#pragma unroll
        for (int r = 0; r < 4; ++r) { const float v = bf_round(acc1[a][c][r]); gate[a][c][r] = bf_round(v * __builtin_amdgcn_rcpf(1.f + __expf(-v))); }
#pragma unroll
    for (int a = 0; a < 2; ++a) { pin(gate[a][0]); pin(gate[a][1]); }
    tbar();
    bf16x8 gf[2][4], bf[2][4];
    const int go = lane * 16 + (w & 1) * 4096, bs = ta ^ ((w & 2) << 5);
    auto rd5 = [&](int buf, int b) {
#pragma unroll
      for (int k = 0; k < 2; ++k)
#pragma unroll
        for (int c = 0; c < 2; ++c) {
          gf[buf][k * 2 + c] = lds_b128(lds + ((U_G + b) & 15) * UNIT + go + (c * 2 + k) * 1024);
          bf[buf][k * 2 + c] = lds_tr2(lds + ((U_END + b) & 15) * UNIT + (bs ^ (c << 5)) + k * 4096);
        }
    };
    rd5(0, 4);
#pragma unroll
    for (int a = 0; a < 2; ++a) { acc2[a][0] = z4; acc2[a][1] = z4; }
    sfor<8>([&](auto B) {
      constexpr int q = B, b = (q + 4) & 7, cur = q & 1;
      sbar();
      if constexpr (q == 4) { tbar(); rd5(cur, b); sbar(); }
      if constexpr (q != 3 && q < 7) rd5(cur ^ 1, (b + 1) & 7);
#pragma unroll
      for (int k = 0; k < 2; ++k)
#pragma unroll
        for (int a = 0; a < 2; ++a)
#pragma unroll
          for (int c = 0; c < 2; ++c) acc2[a][c] = mfma(gf[cur][k * 2 + c], bf[cur][k * 2 + a], acc2[a][c]);
      if constexpr (q != 3 && q < 7) {
#pragma unroll
        for (int i = 0; i < 4; ++i) { __builtin_amdgcn_sched_group_barrier(0x008, 2, 0); __builtin_amdgcn_sched_group_barrier(0x100, 3, 0); }
      }
      if constexpr (q == 3) { pin(acc2[0][0]); pin(acc2[0][1]); pin(acc2[1][0]); pin(acc2[1][1]); }
    });
    sbar();
  }
#pragma unroll
  for (int a = 0; a < 2; ++a) {
    u32x2 o[2];
#pragma unroll
    for (int c = 0; c < 2; ++c) {
      f32x4 r4;
#pragma unroll
      for (int r = 0; r < 4; ++r) {
        const float sv = bf_round(acc2[a][c][r] * (1.f / 524288.f));
        const float xin = (r & 1) ? bfhi(xv[a][c][r >> 1]) : bflo(xv[a][c][r >> 1]);
        r4[r] = xin + bf_round(sv * gate[a][c][r]);
      }
      o[c] = __builtin_bit_cast(u32x2, pack4(r4));
    }
    auto s0 = __builtin_amdgcn_permlane16_swap(o[0][0], o[1][0], false, false);
    auto s1 = __builtin_amdgcn_permlane16_swap(o[0][1], o[1][1], false, false);
    const u32x4 q = {(unsigned)s0[0], (unsigned)s1[0], (unsigned)s0[1], (unsigned)s1[1]};
    __builtin_amdgcn_raw_buffer_store_b128(q, mk_rsrc(out, S_ * D_ * 2), ((32 * (16 * ((w & 2) + a) + li) + n2t) * 512 + col0 + 16 * (g & 1) + 8 * (g >> 1)) * 2, 0, OUT_AUX);
  }
  TS(63);
  if (w == 0) TS_OUT(0);
}

struct KArgs { const char* x; const char* wr; const char* wi; const char* w1; const char* w2; char* ws; uint16_t* out; };
typedef const __attribute__((address_space(4))) KArgs* kargp;
__global__ __launch_bounds__(448, 2) void fused_k(const uint16_t* __restrict__, const uint16_t* __restrict__, const uint16_t* __restrict__,
                                                   const uint16_t* __restrict__, const uint16_t* __restrict__, char* __restrict__, uint16_t* __restrict__) {
  extern __shared__ __attribute__((aligned(16))) char smem[];
  lptr lds = (lptr)smem;
  u64 pc0, tent = 0;
  kargp ka = (kargp)__builtin_amdgcn_kernarg_segment_ptr();
  asm volatile("s_load_dword s100, %0, 0x0\n\ts_load_dword s101, %0, 0x30" ::"s"(ka) : "s100", "s101");
  asm volatile("s_getpc_b64 %0" : "=s"(pc0));
  const char* code = (const char*)(pc0 & ~(u64)127);
  const int wid = __builtin_amdgcn_readfirstlane(threadIdx.x >> 6), lane = threadIdx.x & 63;
  const int id = blockIdx.x, xcd = id & 7, loc = id >> 3;
  const int rb = 4 * (xcd & 3) + (loc & 3), cg = 8 * (xcd >> 2) + (loc >> 2);
  const int n2t = 4 * xcd + (loc & 3), cb = loc >> 2;
  const int rot = ((loc >> (MAP2 ? 2 : 3)) + loc) & 3;
  if (wid >= 6) { asm volatile("" : "+s"(ka)); sync_role(lds, ka->wr, ka->wi, ka->w1, ka->w2, ka->ws, code, (const char*)ka->out, id, rb, cg, n2t, lane); }
  else if (wid >= 4) { asm volatile("" : "+s"(ka)); dma_role(ka->ws, lds, ka->x, ka->w1, ka->w2, id, rb, cg, n2t, cb, rot, wid - 4, lane); }
  else { asm volatile("" : "+s"(ka)); compute_role(lds, (const uint16_t*)ka->x, ka->ws, ka->out, id, rb, cg, n2t, cb, rot, wid, lane, tent); }
}

extern "C" void kernel_launch(void* const* d_in, const int* in_sizes, int n_in, void* d_out, int out_size, void* d_ws, size_t ws_size, hipStream_t stream) {
  hipFuncSetAttribute((const void*)fused_k, hipFuncAttributeMaxDynamicSharedMemorySize, LDS_BYTES);
  fused_k<<<256, 448, LDS_BYTES, stream>>>((const uint16_t*)d_in[0], (const uint16_t*)d_in[1], (const uint16_t*)d_in[2], (const uint16_t*)d_in[4], (const uint16_t*)d_in[5],
                                           (char*)d_ws, (uint16_t*)d_out);
}
```
